```python
import torch

def _softmax(x, axis=-1):
    return torch.softmax(x, dim=axis)
def _gelu(x):
    return torch.nn.functional.gelu(x, approximate="tanh")
def _silu(x):
    return torch.nn.functional.silu(x)
def _sigmoid(x):
    return torch.sigmoid(x)
def _layernorm(x, axis=-1, eps=1e-5):
    m = x.mean(dim=axis, keepdim=True)
    v = x.var(dim=axis, keepdim=True, unbiased=False)
    return (x - m) / torch.sqrt(v + eps)
def _scatter_add(tgt, idx, src, axis=0):
    return tgt.clone().index_add_(axis, idx, src)
def _segment_sum(src, seg, n):
    out = torch.zeros((n,) + src.shape[1:], dtype=src.dtype)
    return out.index_add_(0, seg, src)
def _pad(x, pw):
    flat = tuple(v for pair in reversed(pw) for v in pair)
    return torch.nn.functional.pad(x, flat)

kernel_name = 'sequential_preLN__gqa_block_cached__dense_activation_silu__decode'

def setup_inputs():

    _w = (lambda a: (lambda t: t.to(torch.bfloat16) if t.is_floating_point() else t)(torch.as_tensor(a)))
    def _A_setup():
        B, Sq, D = 2, 1, 448
        Hq, Hkv, Sk, Dh = 16, 8, 4096, 256
        assert Hq % Hkv == 0, (Hq, Hkv)
        return {
            "x": _w((lambda *s: torch.randn(*s, dtype=torch.bfloat16) * 0.1)(B, Sq, D)),
            "k_cache": _w((lambda *s: torch.randn(*s, dtype=torch.bfloat16) * 1.6)(B, Hkv, Sk, Dh)),
            "v_cache": _w((lambda *s: torch.randn(*s, dtype=torch.bfloat16) * 1.6)(B, Hkv, Sk, Dh)),
            "wq": _w((lambda *s: torch.randn(*s, dtype=torch.bfloat16) * 1.6)(D, Hq * Dh) * (D ** -0.5)),
            "wo": _w((lambda *s: torch.randn(*s, dtype=torch.bfloat16) * 1.6)(Hq * Dh, D) * ((Hq * Dh) ** -0.5)),
        }
    _A_in = _A_setup()

    def _B_setup():
        B, S, D, F = 2, 1, 448, 1792
        return {
            "x": _w((lambda *s: torch.randn(*s, dtype=torch.bfloat16) * 0.1)(B, S, D)),
            "w1": _w((lambda *s: torch.randn(*s, dtype=torch.bfloat16) * 3.2)(D, F) * (D ** -0.5)),
            "w2": _w((lambda *s: torch.randn(*s, dtype=torch.bfloat16) * 3.2)(F, D) * (F ** -0.5)),
        }
    _B_in = _B_setup()

    return {
        "A_x": _A_in["x"],
        "A_k_cache": _A_in["k_cache"],
        "A_v_cache": _A_in["v_cache"],
        "A_wq": _A_in["wq"],
        "A_wo": _A_in["wo"],
        "B_x": _B_in["x"],
        "B_w1": _B_in["w1"],
        "B_w2": _B_in["w2"],
    }

def reference(A_x, A_k_cache, A_v_cache, A_wq, A_wo, B_x, B_w1, B_w2):
    def _A_ref(x, k_cache, v_cache, wq, wo):
        B, Sq, D = x.shape
        Hq, Dh = 16, 256
        Hkv = k_cache.shape[1]
        rep = Hq // Hkv
        q = (lambda x, *d: x.permute(*d))((x @ wq).reshape(B, Sq, Hq, Dh), 0, 2, 1, 3)
        k = (lambda x, n, axis: torch.repeat_interleave(x, n, dim=axis))(k_cache, rep, axis=1)
        v = (lambda x, n, axis: torch.repeat_interleave(x, n, dim=axis))(v_cache, rep, axis=1)
        scores = torch.einsum("bhqd,bhkd->bhqk", q, k) * (Dh ** -0.5)
        p = _softmax(scores, axis=-1)
        o = torch.einsum("bhqk,bhkd->bhqd", p, v)
        return (lambda x, *d: x.permute(*d))(o, 0, 2, 1, 3).reshape(B, Sq, Hq * Dh) @ wo

    def _B_ref(x, w1, w2):
        h = torch.einsum("bsd,df->bsf", x, w1)
        h = _silu(h)
        out = torch.einsum("bsf,fd->bsd", h, w2)
        return out

    x = A_x
    h = x + _A_ref(_layernorm(x, axis=-1), A_k_cache, A_v_cache, A_wq, A_wo)
    h2 = h + _B_ref(_layernorm(h, axis=-1), B_w1, B_w2)
    return h2
```

```cpp
#include <hip/hip_runtime.h>
#include <hip/hip_bf16.h>
#include <stdint.h>

#define ROUND_SCORES 0

typedef unsigned short u16;
typedef unsigned v4u __attribute__((ext_vector_type(4)));
typedef __bf16 bf16x2 __attribute__((ext_vector_type(2)));

constexpr int B = 2, D = 448, HQ = 16, HKV = 8, SK = 4096, DH = 256, F = 1792, NQ = HQ * DH;
constexpr int NSPLIT = 16, KPS = SK / NSPLIT;

__device__ __forceinline__ float bf2f(u16 v) { return __uint_as_float(((unsigned)v) << 16); }
__device__ __forceinline__ float rbf(float f) {
    return __bfloat162float(__float2bfloat16(f));
}
__device__ __forceinline__ float dot2(unsigned a, unsigned b, float acc) {
    return __builtin_amdgcn_fdot2_f32_bf16(__builtin_bit_cast(bf16x2, a), __builtin_bit_cast(bf16x2, b), acc, false);
}
__device__ __forceinline__ float wave_sum(float v) {
    for (int o = 32; o > 0; o >>= 1) v += __shfl_xor(v, o);
    return v;
}
__device__ __forceinline__ float wave_max(float v) {
    for (int o = 32; o > 0; o >>= 1) v = fmaxf(v, __shfl_xor(v, o));
    return v;
}

__device__ void ln_row(const float* in, float* out, int lane) {
    float v[7]; float s = 0.f;
    for (int i = 0; i < 7; ++i) { v[i] = in[lane + 64 * i]; s += v[i]; }
    s = wave_sum(s);
    float mean = s * (1.f / D);
    float ss = 0.f;
    for (int i = 0; i < 7; ++i) { float d = v[i] - mean; ss += d * d; }
    ss = wave_sum(ss);
    float var = ss * (1.f / D);
    float mb = rbf(mean), vb = rbf(var);
    float den = rbf(sqrtf(rbf(vb + 1e-5f)));
    for (int i = 0; i < 7; ++i) out[lane + 64 * i] = rbf(rbf(v[i] - mb) / den);
}

__global__ void __launch_bounds__(256) k_q(const u16* x, const u16* wq, u16* q) {
    __shared__ float xs[2][D], xn[2][D], red[8][2][64];
    int tid = threadIdx.x, lane = tid & 63, wave = tid >> 6;
    for (int i = tid; i < 2 * D; i += 256) xs[i / D][i % D] = bf2f(x[i]);
    __syncthreads();
    if (wave < 2) ln_row(xs[wave], xn[wave], lane);
    __syncthreads();
    int rg = tid >> 5, c2 = tid & 31;
    int col = blockIdx.x * 64 + c2 * 2;
    float a00 = 0, a01 = 0, a10 = 0, a11 = 0;
    for (int r = rg; r < D; r += 8) {
        unsigned w = *(const unsigned*)(wq + (size_t)r * NQ + col);
        float w0 = __uint_as_float(w << 16), w1 = __uint_as_float(w & 0xffff0000u);
        a00 += xn[0][r] * w0; a01 += xn[0][r] * w1; a10 += xn[1][r] * w0; a11 += xn[1][r] * w1;
    }
    red[rg][0][c2 * 2] = a00; red[rg][0][c2 * 2 + 1] = a01; red[rg][1][c2 * 2] = a10; red[rg][1][c2 * 2 + 1] = a11;
    __syncthreads();
    if (tid < 128) {
        int b = tid >> 6, c = tid & 63; float s = 0;
        for (int g = 0; g < 8; ++g) s += red[g][b][c];
        __hip_bfloat16 h = __float2bfloat16(s);
        q[b * NQ + blockIdx.x * 64 + c] = *(u16*)&h;
    }
}

__global__ void __launch_bounds__(256) k_attn(const u16* q, const u16* kc, const u16* vc, float* pacc, float* pml) {
    __shared__ float sc[2][KPS];
    __shared__ float accs[4][2][DH];
    __shared__ float mls[2][2];
    int tid = threadIdx.x, lane = tid & 63, wave = tid >> 6;
    int split = blockIdx.x & 15, bk = blockIdx.x >> 4;
    int b = bk >> 3, kvh = bk & 7;
    int half = lane >> 5, c = lane & 31;
    v4u q0 = *(const v4u*)(q + b * NQ + (kvh * 2) * DH + c * 8);
    v4u q1 = *(const v4u*)(q + b * NQ + (kvh * 2 + 1) * DH + c * 8);
    const u16* kb = kc + ((size_t)bk * SK + split * KPS + wave * 64) * DH;
    const u16* vb = vc + ((size_t)bk * SK + split * KPS + wave * 64) * DH;
    for (int i = 0; i < 32; ++i) {
        v4u k = *(const v4u*)(kb + (size_t)(i * 2 + half) * DH + c * 8);
        float s0 = 0, s1 = 0;
        for (int j = 0; j < 4; ++j) { s0 = dot2(k[j], q0[j], s0); s1 = dot2(k[j], q1[j], s1); }
        for (int o = 16; o > 0; o >>= 1) { s0 += __shfl_xor(s0, o); s1 += __shfl_xor(s1, o); }
        if (c == 0) {
            sc[0][wave * 64 + i * 2 + half] = s0 * 0.0625f; sc[1][wave * 64 + i * 2 + half] = s1 * 0.0625f;
        }
    }
    __syncthreads();
    if (wave < 2) {
        float v[4]; float m = -INFINITY;
        for (int i = 0; i < 4; ++i) { v[i] = sc[wave][lane + 64 * i]; m = fmaxf(m, v[i]); }
        m = wave_max(m);
        float l = 0;
        for (int i = 0; i < 4; ++i) { v[i] = __expf(v[i] - m); l += v[i]; sc[wave][lane + 64 * i] = v[i]; }
        l = wave_sum(l);
        if (lane == 0) { mls[wave][0] = m; mls[wave][1] = l; }
    }
    __syncthreads();
    float a0[8] = {0}, a1[8] = {0};
    for (int i = 0; i < 32; ++i) {
        v4u v = *(const v4u*)(vb + (size_t)(i * 2 + half) * DH + c * 8);
        float p0 = sc[0][wave * 64 + i * 2 + half], p1 = sc[1][wave * 64 + i * 2 + half];
        for (int j = 0; j < 4; ++j) {
            float lo = __uint_as_float(v[j] << 16), hi = __uint_as_float(v[j] & 0xffff0000u);
            a0[2 * j] += p0 * lo; a0[2 * j + 1] += p0 * hi; a1[2 * j] += p1 * lo; a1[2 * j + 1] += p1 * hi;
        }
    }
    for (int j = 0; j < 8; ++j) { a0[j] += __shfl_xor(a0[j], 32); a1[j] += __shfl_xor(a1[j], 32); }
    if (half == 0) for (int j = 0; j < 8; ++j) { accs[wave][0][c * 8 + j] = a0[j]; accs[wave][1][c * 8 + j] = a1[j]; }
    __syncthreads();
    for (int h = 0; h < 2; ++h) {
        float s = accs[0][h][tid] + accs[1][h][tid] + accs[2][h][tid] + accs[3][h][tid];
        pacc[((size_t)(bk * 2 + h) * NSPLIT + split) * DH + tid] = s;
    }
    if (tid < 2) { pml[((bk * 2 + tid) * NSPLIT + split) * 2] = mls[tid][0]; pml[((bk * 2 + tid) * NSPLIT + split) * 2 + 1] = mls[tid][1]; }
}

__global__ void __launch_bounds__(256) k_wo(const float* pacc, const float* pml, const u16* wo, float* ypart) {
    __shared__ float o[2][DH], red[8][2][64];
    int tid = threadIdx.x;
    int h = blockIdx.x / 7, cc = blockIdx.x % 7;
    for (int b = 0; b < 2; ++b) {
        int row = b * HQ + h;
        float M = -INFINITY;
        for (int s = 0; s < NSPLIT; ++s) M = fmaxf(M, pml[(row * NSPLIT + s) * 2]);
        float L = 0, a = 0;
        for (int s = 0; s < NSPLIT; ++s) {
            float w = __expf(pml[(row * NSPLIT + s) * 2] - M);
            L += w * pml[(row * NSPLIT + s) * 2 + 1];
            a += w * pacc[((size_t)row * NSPLIT + s) * DH + tid];
        }
        o[b][tid] = rbf(a / L);
    }
    __syncthreads();
    int rg = tid >> 5, c2 = tid & 31;
    int col = cc * 64 + c2 * 2;
    float a00 = 0, a01 = 0, a10 = 0, a11 = 0;
    for (int r = rg; r < DH; r += 8) {
        unsigned w = *(const unsigned*)(wo + (size_t)(h * DH + r) * D + col);
        float w0 = __uint_as_float(w << 16), w1 = __uint_as_float(w & 0xffff0000u);
        a00 += o[0][r] * w0; a01 += o[0][r] * w1; a10 += o[1][r] * w0; a11 += o[1][r] * w1;
    }
    red[rg][0][c2 * 2] = a00; red[rg][0][c2 * 2 + 1] = a01; red[rg][1][c2 * 2] = a10; red[rg][1][c2 * 2 + 1] = a11;
    __syncthreads();
    if (tid < 128) {
        int b = tid >> 6, c = tid & 63; float s = 0;
        for (int g = 0; g < 8; ++g) s += red[g][b][c];
        ypart[(h * 2 + b) * D + cc * 64 + c] = s;
    }
}

__global__ void __launch_bounds__(256) k_mlp(const u16* x, const float* ypart, const u16* w1, const u16* w2, float* opart, float* hbuf) {
    __shared__ float hs[2][D], hn[2][D], red[8][2][64], act[2][64];
    int tid = threadIdx.x, lane = tid & 63, wave = tid >> 6;
    for (int i = tid; i < 2 * D; i += 256) {
        int b = i / D, c = i % D; float s = 0;
        for (int h = 0; h < 16; ++h) s += ypart[(h * 2 + b) * D + c];
        float hv = rbf(bf2f(x[i]) + rbf(s));
        hs[b][c] = hv;
        if (blockIdx.x == 0) hbuf[i] = hv;
    }
    __syncthreads();
    if (wave < 2) ln_row(hs[wave], hn[wave], lane);
    __syncthreads();
    int rg = tid >> 5, c2 = tid & 31;
    int col = blockIdx.x * 64 + c2 * 2;
    float a00 = 0, a01 = 0, a10 = 0, a11 = 0;
    for (int r = rg; r < D; r += 8) {
        unsigned w = *(const unsigned*)(w1 + (size_t)r * F + col);
        float w0 = __uint_as_float(w << 16), w1f = __uint_as_float(w & 0xffff0000u);
        a00 += hn[0][r] * w0; a01 += hn[0][r] * w1f; a10 += hn[1][r] * w0; a11 += hn[1][r] * w1f;
    }
    red[rg][0][c2 * 2] = a00; red[rg][0][c2 * 2 + 1] = a01; red[rg][1][c2 * 2] = a10; red[rg][1][c2 * 2 + 1] = a11;
    __syncthreads();
    if (tid < 128) {
        int b = tid >> 6, c = tid & 63; float s = 0;
        for (int g = 0; g < 8; ++g) s += red[g][b][c];
        s = rbf(s);
        act[b][c] = rbf(s / (1.f + __expf(-s)));
    }
    __syncthreads();
    if (tid < 224) {
        float a0 = 0, a1 = 0, b0 = 0, b1 = 0;
        for (int r = 0; r < 64; ++r) {
            unsigned w = *(const unsigned*)(w2 + (size_t)(blockIdx.x * 64 + r) * D + tid * 2);
            float w0 = __uint_as_float(w << 16), w1f = __uint_as_float(w & 0xffff0000u);
            a0 += act[0][r] * w0; a1 += act[0][r] * w1f; b0 += act[1][r] * w0; b1 += act[1][r] * w1f;
        }
        opart[(blockIdx.x * 2 + 0) * D + tid * 2] = a0; opart[(blockIdx.x * 2 + 0) * D + tid * 2 + 1] = a1;
        opart[(blockIdx.x * 2 + 1) * D + tid * 2] = b0; opart[(blockIdx.x * 2 + 1) * D + tid * 2 + 1] = b1;
    }
}

__global__ void __launch_bounds__(128) k_fin(const float* opart, const float* hbuf, u16* out) {
    int i = blockIdx.x * 128 + threadIdx.x;
    if (i >= 2 * D) return;
    int b = i / D, c = i % D; float s = 0;
    for (int f = 0; f < 28; ++f) s += opart[(f * 2 + b) * D + c];
    __hip_bfloat16 h = __float2bfloat16(hbuf[i] + rbf(s));
    out[i] = *(u16*)&h;
}

extern "C" void kernel_launch(void* const* d_in, const int* in_sizes, int n_in,
                              void* d_out, int out_size, void* d_ws, size_t ws_size,
                              hipStream_t stream) {
    const u16* x = (const u16*)d_in[0];
    const u16* kc = (const u16*)d_in[1];
    const u16* vc = (const u16*)d_in[2];
    const u16* wq = (const u16*)d_in[3];
    const u16* wo = (const u16*)d_in[4];
    const u16* w1 = (const u16*)d_in[6];
    const u16* w2 = (const u16*)d_in[7];
    char* ws = (char*)d_ws;
    u16* q = (u16*)ws;
    float* pacc = (float*)(ws + 65536);
    float* pml = (float*)(ws + 65536 + 524288);
    float* ypart = (float*)(ws + 1048576);
    float* opart = (float*)(ws + 1048576 + 65536);
    float* hbuf = (float*)(ws + 1048576 + 65536 + 131072);
    k_q<<<64, 256, 0, stream>>>(x, wq, q);
    k_attn<<<256, 256, 0, stream>>>(q, kc, vc, pacc, pml);
    k_wo<<<112, 256, 0, stream>>>(pacc, pml, wo, ypart);
    k_mlp<<<28, 256, 0, stream>>>(x, ypart, w1, w2, opart, hbuf);
    k_fin<<<7, 128, 0, stream>>>(opart, hbuf, (u16*)d_out);
}
```

```cpp
#include <hip/hip_runtime.h>
#include <hip/hip_bf16.h>
#include <stdint.h>

typedef unsigned short u16;
typedef unsigned v4u __attribute__((ext_vector_type(4)));
typedef unsigned v2u __attribute__((ext_vector_type(2)));
typedef float v4f __attribute__((ext_vector_type(4)));
typedef float v2f __attribute__((ext_vector_type(2)));
typedef __bf16 bf16x2 __attribute__((ext_vector_type(2)));
typedef __bf16 bf16x8 __attribute__((ext_vector_type(8)));
#define GAS __attribute__((address_space(1)))

constexpr int DM = 448, NQ = 4096, SK = 4096, DH = 256, FF = 1792;
constexpr int NTHR = 1024;

constexpr unsigned E_OFF = 0;
constexpr unsigned QG_OFF = 4096;
constexpr unsigned AG_OFF = QG_OFF + 131072;
constexpr unsigned ST_OFF = AG_OFF + 524288;
constexpr unsigned YG_OFF = ST_OFF + 65536;
constexpr unsigned OG_OFF = YG_OFF + 57344;
constexpr unsigned WS_USED = OG_OFF + 401408;
constexpr int NF = 56;
#define DRY 1
constexpr unsigned SPIN_CAP = 1u << 17;

#define STAMP(idx) do {} while (0)

constexpr int L_QS = 0;
constexpr int L_TAG = 256;
constexpr int L_ZERO = 260;
constexpr int L_ONES = 264;
constexpr int L_MF = 268;
constexpr int L_ML = 272;
constexpr int L_PA = L_ML + 64;
constexpr int L_QP = L_PA + 256;
constexpr int L_HB = L_QP + 448;
constexpr int L_ST = L_HB + 448;
constexpr int L_AA = L_ST + 128;
constexpr int L_XN = L_AA + 64;
constexpr int L_RP = L_XN + 448;
constexpr int L_MB = L_RP + 2048;
constexpr int L_WT = L_MB + 8192;
constexpr int TILE_DW = 576;
constexpr int L_FL = L_WT + 16 * 2 * TILE_DW;
constexpr int L_HK = L_FL + 16;
constexpr int L_END = L_HK + 4096;

__device__ __forceinline__ float bflo(unsigned v) { return __uint_as_float(v << 16); }
__device__ __forceinline__ float bfhi(unsigned v) { return __uint_as_float(v & 0xffff0000u); }
__device__ __forceinline__ unsigned pkbf(float lo, float hi) {
    bf16x2 r; r[0] = (__bf16)lo; r[1] = (__bf16)hi; return __builtin_bit_cast(unsigned, r);
}
__device__ __forceinline__ float rbf(float f) { return bflo(pkbf(f, f)); }
__device__ __forceinline__ float dot2(unsigned a, unsigned b, float acc) {
    return __builtin_amdgcn_fdot2_f32_bf16(__builtin_bit_cast(bf16x2, a), __builtin_bit_cast(bf16x2, b), acc, false);
}
__device__ __forceinline__ v4f mfma(v4u a, v4u b, v4f c) {
    return __builtin_amdgcn_mfma_f32_16x16x32_bf16(__builtin_bit_cast(bf16x8, a), __builtin_bit_cast(bf16x8, b), c, 0, 0, 0);
}
__device__ __forceinline__ unsigned plo(unsigned r0, unsigned r1) { return __builtin_amdgcn_perm(r1, r0, 0x05040100u); }
__device__ __forceinline__ unsigned phi(unsigned r0, unsigned r1) { return __builtin_amdgcn_perm(r1, r0, 0x07060302u); }
template <int CTRL> __device__ __forceinline__ float dpp(float x) {
    return __builtin_bit_cast(float, __builtin_amdgcn_mov_dpp(__builtin_bit_cast(int, x), CTRL, 0xf, 0xf, true));
}
constexpr int XOR1 = 0xB1, XOR2 = 0x4E, XOR7 = 0x141, ROR8 = 0x128;
__device__ __forceinline__ float tr32(float a, float b) {
    auto s = __builtin_amdgcn_permlane32_swap(__float_as_uint(a), __float_as_uint(b), false, false);
    return __uint_as_float(s[0]) + __uint_as_float(s[1]);
}
__device__ __forceinline__ float tr16(float a, float b) {
    auto s = __builtin_amdgcn_permlane16_swap(__float_as_uint(a), __float_as_uint(b), false, false);
    return __uint_as_float(s[0]) + __uint_as_float(s[1]);
}
template <int CTRL> __device__ __forceinline__ float trdpp(float a, float b, bool bit) {
    const float keep = bit ? b : a, send = bit ? a : b;
    return keep + dpp<CTRL>(send);
}
__device__ __forceinline__ float sum32x(float x) { return tr32(x, x); }
__device__ __forceinline__ float sum16x(float x) { return tr16(x, x); }
__device__ __forceinline__ float max32x(float x) {
    auto s = __builtin_amdgcn_permlane32_swap(__float_as_uint(x), __float_as_uint(x), false, false);
    return fmaxf(__uint_as_float(s[0]), __uint_as_float(s[1]));
}
__device__ __forceinline__ float max16x(float x) {
    auto s = __builtin_amdgcn_permlane16_swap(__float_as_uint(x), __float_as_uint(x), false, false);
    return fmaxf(__uint_as_float(s[0]), __uint_as_float(s[1]));
}
__device__ __forceinline__ float sum_row16(float x) {
    x += dpp<XOR1>(x); x += dpp<XOR2>(x); x += dpp<XOR7>(x); x += dpp<ROR8>(x); return x;
}
__device__ __forceinline__ float max_row16(float x) {
    x = fmaxf(x, dpp<XOR1>(x)); x = fmaxf(x, dpp<XOR2>(x)); x = fmaxf(x, dpp<XOR7>(x)); x = fmaxf(x, dpp<ROR8>(x)); return x;
}
__device__ __forceinline__ float sum_wave(float x) { return sum32x(sum16x(sum_row16(x))); }
__device__ __forceinline__ float rdlane(float x, int l) { return __builtin_bit_cast(float, __builtin_amdgcn_readlane(__builtin_bit_cast(int, x), l)); }

#define BARRIER() asm volatile("s_waitcnt lgkmcnt(0)\n\ts_barrier" ::: "memory")
#define CBAR() asm volatile("" ::: "memory")
#define SCHED() __builtin_amdgcn_sched_barrier(0)

__device__ __forceinline__ v4u ld_nt(const void* p) { return __builtin_nontemporal_load((const GAS v4u*)p); }
__device__ __forceinline__ v2u ld_nt2(const void* p) { return __builtin_nontemporal_load((const GAS v2u*)p); }
__device__ __forceinline__ unsigned ld_nt1(const void* p) { return __builtin_nontemporal_load((const GAS unsigned*)p); }
__device__ __forceinline__ v4u ld_g(__amdgpu_buffer_rsrc_t r, unsigned off) { return __builtin_amdgcn_raw_buffer_load_b128(r, off, 0, 16); }
__device__ __forceinline__ v2u ld_g2(__amdgpu_buffer_rsrc_t r, unsigned off) { return __builtin_amdgcn_raw_buffer_load_b64(r, off, 0, 16); }
__device__ __forceinline__ void st_g(__amdgpu_buffer_rsrc_t r, unsigned off, v4u v) { __builtin_amdgcn_raw_buffer_store_b128(v, r, off, 0, 16); }
__device__ __forceinline__ void st_g2(__amdgpu_buffer_rsrc_t r, unsigned off, v2u v) { __builtin_amdgcn_raw_buffer_store_b64(v, r, off, 0, 16); }
__device__ __forceinline__ bool tagok(v4u v, unsigned tag) { return v[1] == tag && v[3] == tag; }
__device__ __forceinline__ unsigned lds_ld_vol(unsigned* p) { return __hip_atomic_load(p, __ATOMIC_RELAXED, __HIP_MEMORY_SCOPE_WORKGROUP); }

__device__ __forceinline__ void ln_params(float mean, float var, float& mb, float& inv_den) {
    mb = rbf(mean);
    inv_den = __builtin_amdgcn_rcpf(rbf(__builtin_amdgcn_sqrtf(rbf(rbf(var) + 1e-5f))));
}
__device__ __forceinline__ void tile_load(const u16* p, size_t stride, int lane, v4u& r0, v4u& r1) {
    const u16* q = p + (size_t)(lane >> 2) * stride + (lane & 3) * 8;
    r0 = ld_nt(q); r1 = ld_nt(q + 16 * stride);
}
__device__ __forceinline__ void tile_frags(unsigned* t, int lane, v4u r0, v4u r1, v4u& f0, v4u& f1) {
    const int row = lane >> 2;
    unsigned* w = t + row * 16 + (row >> 3) * 16 + (lane & 3) * 4;
    *(v4u*)w = r0; *(v4u*)(w + 288) = r1;
    CBAR();
    const unsigned* r = t + (lane >> 4) * 144 + (lane & 15);
#pragma unroll
    for (int i = 0; i < 4; ++i) {
        const unsigned a = r[32 * i], c = r[32 * i + 16];
        f0[i] = plo(a, c); f1[i] = phi(a, c);
    }
}
__device__ __forceinline__ void repoll(__amdgpu_buffer_rsrc_t r, unsigned off, v4u& v, bool ok) {
    const v4u t = ld_g(r, ok ? 0xFFFFFF00u : off);
#pragma unroll
    for (int i = 0; i < 4; ++i) v[i] = ok ? v[i] : t[i];
}
#define THR_A 32
#define THR_B 0
#define PSLEEP 1
#define POLL_PAUSE() __builtin_amdgcn_s_sleep(PSLEEP)
#define QPOLL_GAP 8

template <int ROLE>
__device__ __forceinline__ void body(const u16* __restrict__ x, const u16* __restrict__ kc, const u16* __restrict__ vc,
                                     const u16* __restrict__ wq, const u16* __restrict__ wo, const u16* __restrict__ w1,
                                     const u16* __restrict__ w2, u16* __restrict__ out, char* ws, unsigned* lds,
                                     const int kvh, const int j, const int wave, const int lane) {
    const __amdgpu_buffer_rsrc_t rs = __builtin_amdgcn_make_buffer_rsrc(ws, 0, WS_USED, 0x00020000);
    float* ldf = (float*)lds;
    STAMP(0);
    __builtin_amdgcn_s_setprio(3);
    const int b = j >> 4, s = j & 15;
    const int n16 = lane & 15, g = lane >> 4;
    const bool bit3 = (lane & 8) != 0, bit4 = (lane & 16) != 0;
    unsigned* wt = lds + L_WT + wave * (2 * TILE_DW);
    const unsigned epoch = __hip_atomic_load((GAS unsigned*)(ws + E_OFF), __ATOMIC_RELAXED, __HIP_MEMORY_SCOPE_AGENT);
    v4u K[8], V[8];
    v4u W0, W1, W2, W3; unsigned xres = 0;
    unsigned stmp;
    {
        const size_t kvrow = (size_t)(b * 8 + kvh) * SK + s * 256;
        const unsigned ko = ((n16 & 7) * DH + ((n16 >> 3) * 4 + g) * 8) * 2, ko2 = ko + 4096;
        const unsigned vo = lane * 16, vo2 = vo + 4096;
        const u16* kb = kc + (kvrow + wave * 16) * DH;
        const u16* vb = vc + (kvrow + wave * 16) * DH;
#define KV_LOADS \
            "global_load_dwordx4 %0, %[ko], %[kb] nt\n\tglobal_load_dwordx4 %1, %[ko], %[kb] offset:128 nt\n\t" \
            "global_load_dwordx4 %2, %[ko], %[kb] offset:256 nt\n\tglobal_load_dwordx4 %3, %[ko], %[kb] offset:384 nt\n\t" \
            "global_load_dwordx4 %4, %[ko2], %[kb] nt\n\tglobal_load_dwordx4 %5, %[ko2], %[kb] offset:128 nt\n\t" \
            "global_load_dwordx4 %6, %[ko2], %[kb] offset:256 nt\n\tglobal_load_dwordx4 %7, %[ko2], %[kb] offset:384 nt\n\t" \
            "global_load_dwordx4 %8, %[vo], %[vb] nt\n\tglobal_load_dwordx4 %9, %[vo], %[vb] offset:1024 nt\n\t" \
            "global_load_dwordx4 %10, %[vo], %[vb] offset:2048 nt\n\tglobal_load_dwordx4 %11, %[vo], %[vb] offset:3072 nt\n\t" \
            "global_load_dwordx4 %12, %[vo2], %[vb] nt\n\tglobal_load_dwordx4 %13, %[vo2], %[vb] offset:1024 nt\n\t" \
            "global_load_dwordx4 %14, %[vo2], %[vb] offset:2048 nt\n\tglobal_load_dwordx4 %15, %[vo2], %[vb] offset:3072 nt\n\t"
#define KV_OUT(c) c(K[0]), c(K[1]), c(K[2]), c(K[3]), c(K[4]), c(K[5]), c(K[6]), c(K[7]), c(V[0]), c(V[1]), c(V[2]), c(V[3]), c(V[4]), c(V[5]), c(V[6]), c(V[7])
        v4u xf = {0, 0, 0, 0}, xa = {0, 0, 0, 0};
        if (wave < 8) {
            const int lc = lane < 56 ? lane : 55;
            const u16* p0 = wave ? wq + (size_t)((j >> 4) * 224 + (wave - 1) * 32 + (lane >> 2)) * NQ + kvh * 512 + (j & 15) * 32 + (lane & 3) * 8 : x + lc * 8;
            const u16* p1 = wave ? p0 + (size_t)16 * NQ : p0 + DM;
            asm volatile("s_cmp_eq_u32 %[wv], 0\n\ts_cbranch_scc1 1f\n\t"
                         "global_load_dwordx4 %0, %2, off nt\n\tglobal_load_dwordx4 %1, %3, off nt\n\ts_branch 2f\n"
                         "1:\n\tglobal_load_dwordx4 %0, %2, off\n\tglobal_load_dwordx4 %1, %3, off\n"
                         "2:" : "=&v"(xf), "=&v"(xa) : "v"(p0), "v"(p1), [wv] "s"(wave) : "memory", "scc");
        }
        SCHED();
        asm volatile("s_barrier" ::: "memory");
        STAMP(13);
        {
            const int i = (wave - 8) & 7;
            const u16* hk = kc + kvrow * DH + (i >> 2) * 8 * DH + (i & 3) * 64;
            const u16* hv = vc + kvrow * DH + i * 512;
            const unsigned lk = (unsigned)(size_t)(__attribute__((address_space(3))) unsigned*)(lds + L_HK + i * 256);
            unsigned keep;
            asm volatile("s_cmp_lt_u32 %[wv], 8\n\ts_cbranch_scc1 1f\n\t"
                         "s_setprio 1\n\t"
                         "s_mov_b32 %[keep], m0\n\ts_mov_b32 m0, %[lk]\n\ts_nop 0\n\tglobal_load_lds_dwordx4 %[ko], %[hk] nt\n\t"
                         "s_add_u32 m0, m0, 0x2000\n\ts_nop 0\n\tglobal_load_lds_dwordx4 %[vo], %[hv] nt\n\ts_mov_b32 m0, %[keep]\n\t"
                         KV_LOADS
                         "1:"
                         : KV_OUT("=&v"), [keep] "=&s"(keep)
                         : [wv] "s"(wave), [ko] "v"(ko), [ko2] "v"(ko2), [vo] "v"(vo), [vo2] "v"(vo2), [kb] "s"(kb), [vb] "s"(vb),
                           [hk] "s"(hk), [hv] "s"(hv), [lk] "s"(lk)
                         : "memory", "scc");
        }
        if (wave < 8) {
            const unsigned tag = epoch + 1u;
            asm volatile("s_waitcnt vmcnt(0)" : "+v"(xf), "+v"(xa));
            if (wave) {
                v4u b0, b1;
                tile_frags(wt, lane, xf, xa, b0, b1);
                asm volatile("" : "+v"(b0), "+v"(b1));
                for (unsigned spins = 0; !__all(lds_ld_vol(lds + L_FL) == tag) && spins < (SPIN_CAP << 6); ++spins) __builtin_amdgcn_s_sleep(1);
                CBAR();
                const v4u a0 = *(const v4u*)(lds + L_XN + (n16 & 1) * (DM / 2) + (j >> 4) * 112 + (wave - 1) * 16 + g * 4);
                const v4f z = {0.f, 0.f, 0.f, 0.f};
                const v4f d0 = mfma(a0, b0, z), d1 = mfma(a0, b1, z);
                if (lane < 16) *(v4f*)(ldf + L_QP + ((wave - 1) * 16 + n16) * 4) = (v4f){d0[0], d1[0], d0[1], d1[1]};
                asm volatile("s_waitcnt lgkmcnt(0)" ::: "memory");
                if (lane == 0) __hip_atomic_store(lds + L_FL + wave, tag, __ATOMIC_RELAXED, __HIP_MEMORY_SCOPE_WORKGROUP);
                STAMP(1);
            } else {
                if (lane < 12) lds[L_ZERO + lane] = (lane >= 4 && lane < 8) ? 0x3f803f80u : 0u;
                const float msk = lane < 56 ? 1.f : 0.f;
                float s0 = 0.f, s1 = 0.f;
#pragma unroll
                for (int k = 0; k < 4; ++k) { s0 += bflo(xf[k]) + bfhi(xf[k]); s1 += bflo(xa[k]) + bfhi(xa[k]); }
                s0 = sum_wave(s0 * msk) * (1.f / DM); s1 = sum_wave(s1 * msk) * (1.f / DM);
                float v0 = 0.f, v1 = 0.f;
#pragma unroll
                for (int k = 0; k < 4; ++k) {
                    float d;
                    d = bflo(xf[k]) - s0; v0 = fmaf(d, d, v0); d = bfhi(xf[k]) - s0; v0 = fmaf(d, d, v0);
                    d = bflo(xa[k]) - s1; v1 = fmaf(d, d, v1); d = bfhi(xa[k]) - s1; v1 = fmaf(d, d, v1);
                }
                v0 = sum_wave(v0 * msk) * (1.f / DM); v1 = sum_wave(v1 * msk) * (1.f / DM);
                float mb0, id0, mb1, id1;
                ln_params(s0, v0, mb0, id0); ln_params(s1, v1, mb1, id1);
                v4u n0, n1;
#pragma unroll
                for (int k = 0; k < 4; ++k) {
                    const unsigned c0 = pkbf(bflo(xf[k]) - mb0, bfhi(xf[k]) - mb0), c1 = pkbf(bflo(xa[k]) - mb1, bfhi(xa[k]) - mb1);
                    n0[k] = pkbf(bflo(c0) * id0, bfhi(c0) * id0);
                    n1[k] = pkbf(bflo(c1) * id1, bfhi(c1) * id1);
                }
                if (lane < 56) { *(v4u*)(lds + L_XN + lane * 4) = n0; *(v4u*)(lds + L_XN + DM / 2 + lane * 4) = n1; }
                asm volatile("s_waitcnt lgkmcnt(0)" ::: "memory");
                if (lane == 0) __hip_atomic_store(lds + L_FL, tag, __ATOMIC_RELAXED, __HIP_MEMORY_SCOPE_WORKGROUP);
                STAMP(12);
                for (unsigned spins = 0; !__all(lds_ld_vol(lds + L_FL + (lane & 7)) == tag) && spins < (SPIN_CAP << 6); ++spins) {}
                CBAR();
                {
                    v2f t0 = {0.f, 0.f};
#pragma unroll
                    for (int w = 0; w < 7; ++w) t0 += *(const v2f*)(ldf + L_QP + (w * 16 + n16) * 4 + (g & 1) * 2);
                    const v4u gq = {__float_as_uint(t0[0]), tag, __float_as_uint(t0[1]), tag};
                    if (lane < 32) st_g(rs, QG_OFF + ((kvh * 32 + j) * 32 + lane) * 16, gq);
                }
                STAMP(1);
                const unsigned base = QG_OFF + ((kvh * 32 + g) * 32 + b * 16 + n16) * 16;
                v4u gg[8];
                __builtin_amdgcn_s_sleep(QPOLL_GAP);
                for (unsigned spins = 0;; ++spins) {
                    CBAR();
                    bool ok = true;
#pragma unroll
                    for (int i = 0; i < 8; ++i) gg[i] = ld_g(rs, base + i * 2048);
#pragma unroll
                    for (int i = 0; i < 8; ++i) ok &= tagok(gg[i], tag);
                    if (__all(ok) || spins > SPIN_CAP) break;
                }
#pragma unroll
                for (int m = 0; m < 4; ++m)
                    lds[L_QS + m * 64 + lane] = pkbf(__uint_as_float(gg[m][0]) + __uint_as_float(gg[4 + m][0]), __uint_as_float(gg[m][2]) + __uint_as_float(gg[4 + m][2]));
            }
            __builtin_amdgcn_s_waitcnt(0x0F70);
        }
        asm volatile("s_sub_u32 %[t], %[wv], 1\n\ts_cmp_gt_u32 %[t], 6\n\ts_cbranch_scc1 1f\n\t"
                     "s_setprio 1\n\t"
                     KV_LOADS
                     "1:\n\ts_setprio 0\n\t"
                     "s_waitcnt vmcnt(16) lgkmcnt(0)\n\ts_barrier\n\t"
                     "s_cmp_lg_u32 %[wv], 0\n\ts_cbranch_scc1 2f\n\t"
                     "ds_read_b128 %0, %[hl]\n\tds_read_b128 %1, %[hl] offset:1024\n\tds_read_b128 %2, %[hl] offset:2048\n\tds_read_b128 %3, %[hl] offset:3072\n\t"
                     "ds_read_b128 %4, %[hl] offset:4096\n\tds_read_b128 %5, %[hl] offset:5120\n\tds_read_b128 %6, %[hl] offset:6144\n\tds_read_b128 %7, %[hl] offset:7168\n\t"
                     "ds_read_b128 %8, %[hl] offset:8192\n\tds_read_b128 %9, %[hl] offset:9216\n\tds_read_b128 %10, %[hl] offset:10240\n\tds_read_b128 %11, %[hl] offset:11264\n\t"
                     "ds_read_b128 %12, %[hl] offset:12288\n\tds_read_b128 %13, %[hl] offset:13312\n\tds_read_b128 %14, %[hl] offset:14336\n\tds_read_b128 %15, %[hl] offset:15360\n\t"
                     "s_waitcnt lgkmcnt(0)\n\t"
                     "2:"
                     : KV_OUT("+v"), [t] "=&s"(stmp)
                     : [wv] "s"(wave), [ko] "v"(ko), [ko2] "v"(ko2), [vo] "v"(vo), [vo2] "v"(vo2), [kb] "s"(kb), [vb] "s"(vb),
                       [hl] "v"((unsigned)(size_t)(__attribute__((address_space(3))) unsigned*)(lds + L_HK) + lane * 16)
                     : "memory", "scc");
        STAMP(2);
    }
    float t;
    {
        const unsigned* qa = lds + L_QS + (n16 & 1) * 128 + (((n16 >> 1) & 1) * 4 + g) * 4;
        v4f d0 = {0.f, 0.f, 0.f, 0.f}, d1 = {0.f, 0.f, 0.f, 0.f};
        v4u qf[4];
#pragma unroll
        for (int i = 0; i < 4; ++i) qf[i] = *(const v4u*)(qa + i * 32);
        asm volatile("s_waitcnt vmcnt(8)" : "+v"(K[0]), "+v"(K[1]), "+v"(K[2]), "+v"(K[3]), "+v"(K[4]), "+v"(K[5]), "+v"(K[6]), "+v"(K[7]));
#pragma unroll
        for (int i = 0; i < 4; ++i) { d0 = mfma(qf[i], K[i], d0); d1 = mfma(qf[i], K[4 + i], d1); }
        const float s0 = trdpp<ROR8>(bit3 ? d0[2] : d0[0], bit3 ? d1[2] : d1[0], bit3);
        const float s1 = trdpp<ROR8>(bit3 ? d0[3] : d0[1], bit3 ? d1[3] : d1[1], bit3);
        t = rbf(bit4 ? s1 : s0) * (0.0625f * 1.4426950408889634f);
    }
    {
        const float mw = max_row16(t);
        if ((lane & 47) == 0) ldf[L_ML + (lane >> 4) * 16 + wave] = mw;
    }
    BARRIER();
    {
        const float M = max_row16(ldf[L_ML + (g & 1) * 16 + n16]);
        const float p = __builtin_amdgcn_exp2f(t - M);
        if (lane < 32) ((u16*)(lds + L_PA + wave * 16))[(g * 2 + (n16 & 1)) * 8 + (n16 >> 1)] = (u16)pkbf(p, p);
        CBAR();
        const float lw = sum_row16(p);
        if ((lane & 47) == 0) { ldf[L_ML + 32 + (lane >> 4) * 16 + wave] = lw; if (wave == 0) ldf[L_MF + (lane >> 4)] = M; }
    }
    v4u PA[4];
#pragma unroll
    for (int tb = 0; tb < 4; ++tb) {
        const bool on = (n16 >> 2) == tb && ((n16 >> 1) & 1) == (g & 1);
        PA[tb] = *(const v4u*)(lds + (on ? L_PA + wave * 16 + ((n16 & 1) * 2 + (g >> 1)) * 4 : L_ZERO));
    }
    STAMP(3);
    {
        v4f acc0 = {0.f, 0.f, 0.f, 0.f}, acc1 = {0.f, 0.f, 0.f, 0.f};
        asm volatile("s_waitcnt vmcnt(0)" : "+v"(V[0]), "+v"(V[1]), "+v"(V[2]), "+v"(V[3]), "+v"(V[4]), "+v"(V[5]), "+v"(V[6]), "+v"(V[7]));
#pragma unroll
        for (int tt = 0; tt < 8; ++tt) {
            v4u bt;
#pragma unroll
            for (int ii = 0; ii < 4; ++ii)
                bt[ii] = (tt & 1) ? phi(V[2 * ii][tt >> 1], V[2 * ii + 1][tt >> 1]) : plo(V[2 * ii][tt >> 1], V[2 * ii + 1][tt >> 1]);
            if (tt < 4) acc0 = mfma(PA[tt & 3], bt, acc0); else acc1 = mfma(PA[tt & 3], bt, acc1);
        }
        *(v4f*)(ldf + L_MB + ((wave * 2 + 0) * 64 + lane) * 4) = acc0;
        *(v4f*)(ldf + L_MB + ((wave * 2 + 1) * 64 + lane) * 4) = acc1;
        asm volatile("s_waitcnt lgkmcnt(0)" ::: "memory");
        if (lane == 0) __hip_atomic_fetch_add(lds + L_MF + 2, 1u, __ATOMIC_RELAXED, __HIP_MEMORY_SCOPE_WORKGROUP);
    }
    STAMP(4);
    unsigned tag = epoch;
    asm volatile("v_add_u32 %0, 1, %0" : "+v"(tag));
    if constexpr (ROLE == 1) {
        if (wave >= 4 && wave < 12) {
            const int h = kvh * 2 + j / 7, cc = j % 7;
            const u16* p = wo + (size_t)(h * 256 + (wave - 4) * 32 + (lane >> 3)) * DM + cc * 64 + (lane & 7) * 8;
            W0 = ld_nt(p); W1 = ld_nt(p + 8 * DM); W2 = ld_nt(p + 16 * DM); W3 = ld_nt(p + 24 * DM);
            unsigned* w = lds + L_WT + ((wave - 4) * 32 + (lane >> 3)) * 32 + (lane & 7) * 4;
            *(v4u*)w = W0; *(v4u*)(w + 8 * 32) = W1; *(v4u*)(w + 16 * 32) = W2; *(v4u*)(w + 24 * 32) = W3;
        }
    }
    SCHED();
    if (wave < 4) {
        __builtin_amdgcn_s_setprio(3);
        for (unsigned spins = 0; lds_ld_vol(lds + L_MF + 2) < 16u && spins < (SPIN_CAP << 8); ++spins) {}
        CBAR();
        const int lam = wave * 16 + n16;
        v4f S0 = {0.f, 0.f, 0.f, 0.f}, S1 = {0.f, 0.f, 0.f, 0.f};
        const float lw = ldf[L_ML + 32 + (g >> 1) * 16 + n16];
        const float M = ldf[L_MF + (g >> 1)];
#pragma unroll
        for (int k = 0; k < 4; ++k) {
            S0 += *(const v4f*)(ldf + L_MB + (((g * 4 + k) * 2 + 0) * 64 + lam) * 4);
            S1 += *(const v4f*)(ldf + L_MB + (((g * 4 + k) * 2 + 1) * 64 + lam) * 4);
        }
        const float L = sum_row16(lw);
        const float inv = __builtin_amdgcn_rcpf(L);
        const float e0 = sum16x(tr32(S0[0], S0[1])) * inv, e1 = sum16x(tr32(S1[0], S1[1])) * inv;
        const float e2 = sum16x(tr32(S0[2], S0[3])) * inv, e3 = sum16x(tr32(S1[2], S1[3])) * inv;
        const int hg = kvh * 2 + (g >> 1);
        if (!bit4) {
            const v4u gv = {pkbf(e0, e1), tag, pkbf(e2, e3), tag};
            st_g(rs, AG_OFF + (((hg * 2 + b) * 16 + s) * 64 + lam) * 16, gv);
        } else if (wave == 0 && n16 == 0) {
            const v2u gs = {__float_as_uint(M + __builtin_amdgcn_logf(L)), tag};
            st_g2(rs, ST_OFF + ((hg * 2 + b) * 16 + s) * 128, gs);
        }
        __builtin_amdgcn_s_setprio(0);
    }
    STAMP(5);
    if constexpr (ROLE == 2) {
        const int f = (j - 14) * 8 + kvh;
        const int wr = wave < 14 ? wave : 13;
        tile_load(w1 + (size_t)(wr * 32) * FF + f * 32, FF, lane, W0, W1);
        tile_load(w2 + (size_t)(f * 32) * DM + wr * 32, DM, lane, W2, W3);
        xres = *(const GAS u16*)(x + ((lane >> 3) & 1) * DM + wr * 32 + (lane & 7) * 4 + g);
        SCHED();
    }
    if constexpr (ROLE == 1) {
        const int h = kvh * 2 + j / 7, cc = j % 7, kk = wave >> 1, bb = wave & 1;
        const unsigned a0 = AG_OFF + ((((h * 2 + bb) * 16 + n16) * 8 + kk) * 8 + 2 * g) * 16;
        const unsigned a2 = ST_OFF + ((h * 2 + bb) * 16 + n16) * 128;
        if (wave == 15) {
            const unsigned af = ST_OFF + (h * 32 + (lane & 31)) * 128;
            for (unsigned spins = 0;; ++spins) {
                CBAR();
                const v2u fl = ld_g2(rs, af);
                if (__builtin_popcountll(__ballot(fl[1] != tag) & 0xffffffffull) <= THR_A || spins > SPIN_CAP) break;
                __builtin_amdgcn_s_sleep(2);
            }
        }
        STAMP(10);
        BARRIER();
        STAMP(11);
        v4u d0 = ld_g(rs, a0), d1 = ld_g(rs, a0 + 16);
        v2u st0 = ld_g2(rs, a2);
        SCHED();
        v4u B[4];
#pragma unroll
        for (int i = 0; i < 4; ++i) {
            const int lam = 8 * kk + 2 * g + (i >> 1), e = (2 * i) & 3;
            const int row = 128 * (e >> 1) + 8 * (lam & 15) + (lam >> 4);
            const v2u r0 = *(const v2u*)(lds + L_WT + row * 32 + 2 * n16), r1 = *(const v2u*)(lds + L_WT + (row + 4) * 32 + 2 * n16);
            B[0][i] = plo(r0[0], r1[0]); B[1][i] = phi(r0[0], r1[0]);
            B[2][i] = plo(r0[1], r1[1]); B[3][i] = phi(r0[1], r1[1]);
        }
        asm volatile("" : "+v"(B[0]), "+v"(B[1]), "+v"(B[2]), "+v"(B[3]));
        for (unsigned spins = 0;; ++spins) {
            const bool ok = tagok(d0, tag) && tagok(d1, tag) && st0[1] == tag;
            if (__all(ok) || spins > SPIN_CAP) break;
            POLL_PAUSE();
            CBAR();
            repoll(rs, a0, d0, ok); repoll(rs, a0 + 16, d1, ok);
            const v2u t2 = ld_g2(rs, ok ? 0xFFFFFF00u : a2);
            st0[0] = ok ? st0[0] : t2[0]; st0[1] = ok ? st0[1] : t2[1];
        }
        STAMP(6);
        const v4u a = {d0[0], d0[2], d1[0], d1[2]};
        const v4f z = {0.f, 0.f, 0.f, 0.f};
        const v4f D0 = mfma(a, B[0], z), D1 = mfma(a, B[1], z), D2 = mfma(a, B[2], z), D3 = mfma(a, B[3], z);
        const float ls = __uint_as_float(st0[0]);
        const float fe = __builtin_amdgcn_exp2f(ls - max_row16(ls));
        const int fi = __float_as_int(fe);
        const float f0 = __int_as_float(__builtin_amdgcn_ds_bpermute(16 * g + 0, fi)), f1 = __int_as_float(__builtin_amdgcn_ds_bpermute(16 * g + 4, fi));
        const float f2 = __int_as_float(__builtin_amdgcn_ds_bpermute(16 * g + 8, fi)), f3 = __int_as_float(__builtin_amdgcn_ds_bpermute(16 * g + 12, fi));
        const float inv = __builtin_amdgcn_rcpf(sum_row16(fe));
        const float y0 = fmaf(f3, D0[3], fmaf(f2, D0[2], fmaf(f1, D0[1], f0 * D0[0])));
        const float y1 = fmaf(f3, D1[3], fmaf(f2, D1[2], fmaf(f1, D1[1], f0 * D1[0])));
        const float y2 = fmaf(f3, D2[3], fmaf(f2, D2[2], fmaf(f1, D2[1], f0 * D2[0])));
        const float y3 = fmaf(f3, D3[3], fmaf(f2, D3[2], fmaf(f1, D3[1], f0 * D3[0])));
        const float y = tr16(tr32(y0, y2), tr32(y1, y3)) * inv;
        ldf[L_RP + wave * 64 + 4 * n16 + g] = y;
        BARRIER();
        if (wave == 0) {
            const int b2 = g & 1;
            v4f acc = {0.f, 0.f, 0.f, 0.f};
#pragma unroll
            for (int k = 0; k < 8; ++k) acc += *(const v4f*)(ldf + L_RP + (2 * k + b2) * 64 + 4 * n16);
            const v4u gv = {pkbf(acc[0], acc[1]), tag, pkbf(acc[2], acc[3]), tag};
            if (lane < 32) st_g(rs, YG_OFF + ((((h * 7 + cc) * 2 + (n16 >> 3)) * 2 + b2) * 8 + (n16 & 7)) * 16, gv);
        }
        STAMP(7);
    } else if constexpr (ROLE == 2) {
        const int f = (j - 14) * 8 + kvh;
        v4u B1[2], B2[2];
        tile_frags(wt, lane, W0, W1, B1[0], B1[1]);
        tile_frags(wt + TILE_DW, lane, W2, W3, B2[0], B2[1]);
        asm volatile("" : "+v"(B1[0]), "+v"(B1[1]), "+v"(B2[0]), "+v"(B2[1]), "+v"(xres));
        const v4f z4 = {0.f, 0.f, 0.f, 0.f};
        unsigned klo = 0x00003f80u, khi = 0x3f800000u;
        asm volatile("" : "+v"(klo), "+v"(khi));
        if (wave == 15) {
            const unsigned af = YG_OFF + ((lane + f) % 112) * 512;
            for (unsigned spins = 0;; ++spins) {
                CBAR();
                const v2u fl = ld_g2(rs, af);
                if (__builtin_popcountll(__ballot(fl[1] == tag)) >= THR_B || spins > SPIN_CAP) break;
                __builtin_amdgcn_s_sleep(2);
            }
        }
        STAMP(10);
        BARRIER();
        STAMP(11);
        if (wave < 14) {
            const unsigned ay = YG_OFF + (((g * 7 + (wave >> 1)) * 2 + (wave & 1)) * 16 + n16) * 16;
            constexpr unsigned HS = 4 * 7 * 512;
            v4u y[4];
#pragma unroll
            for (int i = 0; i < 4; ++i) y[i] = ld_g(rs, ay + i * HS);
            for (unsigned spins = 0;; ++spins) {
                const bool k0 = tagok(y[0], tag), k1 = tagok(y[1], tag), k2 = tagok(y[2], tag), k3 = tagok(y[3], tag);
                if (__all(k0 && k1 && k2 && k3) || spins > SPIN_CAP) break;
                POLL_PAUSE();
                CBAR();
                repoll(rs, ay, y[0], k0); repoll(rs, ay + HS, y[1], k1); repoll(rs, ay + 2 * HS, y[2], k2); repoll(rs, ay + 3 * HS, y[3], k3);
            }
            STAMP(6);
            float c0 = 0.f, c1 = 0.f, c2 = 0.f, c3 = 0.f;
#pragma unroll
            for (int i = 0; i < 4; ++i) {
                c0 = dot2(y[i][0], klo, c0); c1 = dot2(y[i][0], khi, c1);
                c2 = dot2(y[i][2], klo, c2); c3 = dot2(y[i][2], khi, c3);
            }
            const float ys = tr16(tr32(c0, c2), tr32(c1, c3));
            const unsigned hb16 = pkbf(bflo(xres) + rbf(ys), 0.f);
            const float hv = bflo(hb16);
            ((u16*)(lds + L_HB))[(bit3 ? DM : 0) + wave * 32 + (lane & 7) * 4 + g] = (u16)hb16;
            CBAR();
            const v4u av = *(const v4u*)(lds + (n16 == 2 ? L_ONES : L_HB + (n16 & 1) * (DM / 2) + wave * 16 + g * 4));
            const float h0 = bit3 ? rdlane(hv, 8) : rdlane(hv, 0);
            const float dd = hv - h0;
            float sd = dd, sq = dd * dd;
            sd += dpp<XOR1>(sd); sq += dpp<XOR1>(sq); sd += dpp<XOR2>(sd); sq += dpp<XOR2>(sq); sd += dpp<XOR7>(sd); sq += dpp<XOR7>(sq);
            sd = sum16x(sd); sq = sum16x(sq); sd = sum32x(sd); sq = sum32x(sq);
            const v4f D0 = mfma(av, B1[0], z4), D1 = mfma(av, B1[1], z4);
            if ((lane & 55) == 0) *(v4f*)(ldf + L_ST + ((lane >> 3) * 16 + wave) * 4) = (v4f){h0, sd, sq, 0.f};
            if (lane < 16) {
                float* zp = ldf + L_RP + (wave * 32 + n16) * 4;
                *(v4f*)zp = D0; *(v4f*)(zp + 64) = D1;
            }
        }
        BARRIER();
        if (wave != 0) STAMP(12);
        if (wave < 2) {
            const float* zp = ldf + L_RP + (wave * 16 + n16) * 4;
            const v4f sv = *(const v4f*)(ldf + L_ST + ((g >> 1) * 16 + (n16 < 14 ? n16 : 13)) * 4);
            v4f zs = *(const v4f*)(zp + g * 128) + *(const v4f*)(zp + (g + 4) * 128) + *(const v4f*)(zp + (g + 8) * 128);
            const v4f zl = *(const v4f*)(g < 2 ? zp + (g + 12) * 128 : ldf + L_ZERO);
            zs += zl;
            const float valid = n16 < 14 ? 1.f : 0.f;
            const float mw = fmaf(sv[1], 1.f / 32.f, sv[0]);
            const float mean = sum_row16(mw * valid) * (1.f / 14.f);
            const float dm = mw - mean;
            const float m2 = fmaf(32.f * dm, dm, fmaf(-sv[1] * (1.f / 32.f), sv[1], sv[2]));
            const float var = sum_row16(m2 * valid) * (1.f / DM);
            float mb, idn;
            ln_params(mean, fmaxf(var, 0.f), mb, idn);
            const float zb = sum16x(tr32(zs[0], zs[1])), cs = sum16x(sum32x(zs[2]));
            const float zz = rbf((zb - mb * cs) * idn);
            const float act = zz * __builtin_amdgcn_rcpf(1.f + __builtin_amdgcn_exp2f(zz * -1.4426950408889634f));
            if (!bit4) ((u16*)(lds + L_AA))[(g >> 1) * 32 + 2 * n16 + wave] = (u16)pkbf(act, act);
        }
        BARRIER();
        if (wave < 14) {
            __builtin_amdgcn_s_setprio(3);
            const v4u a0 = *(const v4u*)(lds + L_AA + (n16 & 1) * 16 + g * 4);
            const v4f D0 = mfma(a0, B2[0], z4), D1 = mfma(a0, B2[1], z4);
            const int cp = wave * 16 + n16;
            const unsigned o = OG_OFF + ((f * NF + (cp >> 2)) * 8 + (cp & 3)) * 16;
            const v4u g0 = {__float_as_uint(D0[0]), tag, __float_as_uint(D1[0]), tag};
            const v4u g1 = {__float_as_uint(D0[1]), tag, __float_as_uint(D1[1]), tag};
            if (lane < 16) { st_g(rs, o, g0); st_g(rs, o + 64, g1); }
            __builtin_amdgcn_s_setprio(0);
        }
        STAMP(7);
        if (wave == 15) {
            const unsigned a = OG_OFF + (((lane >> 3) * NF + f) * 8 + (lane & 7)) * 16;
            constexpr unsigned FS = 8 * NF * 128;
            const int col = ((lane >> 2) & 1) * DM + f * 8 + 2 * (lane & 3);
            const unsigned hh2 = lds[L_HB + col / 2];
            v4u gv[7];
#pragma unroll
            for (int i = 0; i < 7; ++i) gv[i] = ld_g(rs, a + i * FS);
            for (unsigned spins = 0;; ++spins) {
                bool k[7], ok = true;
#pragma unroll
                for (int i = 0; i < 7; ++i) { k[i] = tagok(gv[i], tag); ok &= k[i]; }
                if (__all(ok) || spins > SPIN_CAP) break;
                CBAR();
#pragma unroll
                for (int i = 0; i < 7; ++i) repoll(rs, a + i * FS, gv[i], k[i]);
            }
            STAMP(8);
            float s0 = 0.f, s1 = 0.f;
#pragma unroll
            for (int i = 0; i < 7; ++i) { s0 += __uint_as_float(gv[i][0]); s1 += __uint_as_float(gv[i][2]); }
            s0 += dpp<ROR8>(s0); s1 += dpp<ROR8>(s1);
            s0 = sum32x(sum16x(s0)); s1 = sum32x(sum16x(s1));
            if (lane < 8) {
                *(GAS unsigned*)(out + col) = pkbf(bflo(hh2) + rbf(s0), bfhi(hh2) + rbf(s1));
                if (f == 0 && lane == 0) __hip_atomic_store((GAS unsigned*)(ws + E_OFF), tag, __ATOMIC_RELAXED, __HIP_MEMORY_SCOPE_AGENT);
            }
        }
        STAMP(9);
    }
}

__global__ void __launch_bounds__(NTHR, 4)
fused_decode(const u16* __restrict__ x, const u16* __restrict__ kc, const u16* __restrict__ vc, const u16* __restrict__ wq,
             const u16* __restrict__ wo, const u16* __restrict__ w1, const u16* __restrict__ w2, u16* __restrict__ out, char* ws) {
    __shared__ __attribute__((aligned(16))) unsigned lds[L_END];
    const int wave = __builtin_amdgcn_readfirstlane(threadIdx.x >> 6), lane = threadIdx.x & 63;
    const int kvh = blockIdx.x & 7, j = blockIdx.x >> 3;
    const int role = j < 14 ? 1 : (j < 21 ? 2 : 0);
    if (role == 1) body<1>(x, kc, vc, wq, wo, w1, w2, out, ws, lds, kvh, j, wave, lane);
    else if (role == 2) body<2>(x, kc, vc, wq, wo, w1, w2, out, ws, lds, kvh, j, wave, lane);
    else body<0>(x, kc, vc, wq, wo, w1, w2, out, ws, lds, kvh, j, wave, lane);
}

extern "C" void kernel_launch(void* const* d_in, const int* in_sizes, int n_in, void* d_out, int out_size, void* d_ws,
                              size_t ws_size, hipStream_t stream) {
    fused_decode<<<256, NTHR, 0, stream>>>((const u16*)d_in[0], (const u16*)d_in[1], (const u16*)d_in[2], (const u16*)d_in[3],
                                           (const u16*)d_in[4], (const u16*)d_in[6], (const u16*)d_in[7], (u16*)d_out, (char*)d_ws);
}
```

```cpp
#include <hip/hip_runtime.h>
#include <hip/hip_bf16.h>
#include <stdint.h>

typedef unsigned short u16;
typedef unsigned v4u __attribute__((ext_vector_type(4)));
typedef unsigned v2u __attribute__((ext_vector_type(2)));
typedef float v4f __attribute__((ext_vector_type(4)));
typedef float v2f __attribute__((ext_vector_type(2)));
typedef __bf16 bf16x2 __attribute__((ext_vector_type(2)));
typedef __bf16 bf16x8 __attribute__((ext_vector_type(8)));
#define GAS __attribute__((address_space(1)))

constexpr int DM = 448, NQ = 4096, SK = 4096, DH = 256, FF = 1792;
constexpr int NTHR = 1024;

constexpr unsigned E_OFF = 0;
constexpr unsigned QG_OFF = 4096;
constexpr unsigned AG_OFF = QG_OFF + 131072;
constexpr unsigned ST_OFF = AG_OFF + 524288;
constexpr unsigned YG_OFF = ST_OFF + 65536;
constexpr unsigned OG_OFF = YG_OFF + 57344;
constexpr unsigned WS_USED = OG_OFF + 401408;
constexpr int NF = 56;
#define DRY 1
constexpr unsigned SPIN_CAP = 1u << 17;

#define STAMP(idx) do {} while (0)

constexpr int L_QS = 0;
constexpr int L_TAG = 256;
constexpr int L_ZERO = 260;
constexpr int L_ONES = 264;
constexpr int L_MF = 268;
constexpr int L_ML = 272;
constexpr int L_PA = L_ML + 64;
constexpr int L_QP = L_PA + 256;
constexpr int L_HB = L_QP + 448;
constexpr int L_ST = L_HB + 448;
constexpr int L_AA = L_ST + 128;
constexpr int L_XN = L_AA + 64;
constexpr int L_RP = L_XN + 448;
constexpr int L_MB = L_RP + 2048;
constexpr int L_WT = L_MB + 8192;
constexpr int TILE_DW = 576;
constexpr int L_FL = L_WT + 16 * 2 * TILE_DW;
constexpr int L_HK = L_FL + 16;
constexpr int L_END = L_HK + 4096;

__device__ __forceinline__ float bflo(unsigned v) { return __uint_as_float(v << 16); }
__device__ __forceinline__ float bfhi(unsigned v) { return __uint_as_float(v & 0xffff0000u); }
__device__ __forceinline__ unsigned pkbf(float lo, float hi) {
    bf16x2 r; r[0] = (__bf16)lo; r[1] = (__bf16)hi; return __builtin_bit_cast(unsigned, r);
}
__device__ __forceinline__ float rbf(float f) { return bflo(pkbf(f, f)); }
__device__ __forceinline__ float dot2(unsigned a, unsigned b, float acc) {
    return __builtin_amdgcn_fdot2_f32_bf16(__builtin_bit_cast(bf16x2, a), __builtin_bit_cast(bf16x2, b), acc, false);
}
__device__ __forceinline__ v4f mfma(v4u a, v4u b, v4f c) {
    return __builtin_amdgcn_mfma_f32_16x16x32_bf16(__builtin_bit_cast(bf16x8, a), __builtin_bit_cast(bf16x8, b), c, 0, 0, 0);
}
__device__ __forceinline__ unsigned plo(unsigned r0, unsigned r1) { return __builtin_amdgcn_perm(r1, r0, 0x05040100u); }
__device__ __forceinline__ unsigned phi(unsigned r0, unsigned r1) { return __builtin_amdgcn_perm(r1, r0, 0x07060302u); }
template <int CTRL> __device__ __forceinline__ float dpp(float x) {
    return __builtin_bit_cast(float, __builtin_amdgcn_mov_dpp(__builtin_bit_cast(int, x), CTRL, 0xf, 0xf, true));
}
constexpr int XOR1 = 0xB1, XOR2 = 0x4E, XOR7 = 0x141, ROR8 = 0x128;
__device__ __forceinline__ float tr32(float a, float b) {
    auto s = __builtin_amdgcn_permlane32_swap(__float_as_uint(a), __float_as_uint(b), false, false);
    return __uint_as_float(s[0]) + __uint_as_float(s[1]);
}
__device__ __forceinline__ float tr16(float a, float b) {
    auto s = __builtin_amdgcn_permlane16_swap(__float_as_uint(a), __float_as_uint(b), false, false);
    return __uint_as_float(s[0]) + __uint_as_float(s[1]);
}
template <int CTRL> __device__ __forceinline__ float trdpp(float a, float b, bool bit) {
    const float keep = bit ? b : a, send = bit ? a : b;
    return keep + dpp<CTRL>(send);
}
__device__ __forceinline__ float sum32x(float x) { return tr32(x, x); }
__device__ __forceinline__ float sum16x(float x) { return tr16(x, x); }
__device__ __forceinline__ float max32x(float x) {
    auto s = __builtin_amdgcn_permlane32_swap(__float_as_uint(x), __float_as_uint(x), false, false);
    return fmaxf(__uint_as_float(s[0]), __uint_as_float(s[1]));
}
__device__ __forceinline__ float max16x(float x) {
    auto s = __builtin_amdgcn_permlane16_swap(__float_as_uint(x), __float_as_uint(x), false, false);
    return fmaxf(__uint_as_float(s[0]), __uint_as_float(s[1]));
}
__device__ __forceinline__ float sum_row16(float x) {
    x += dpp<XOR1>(x); x += dpp<XOR2>(x); x += dpp<XOR7>(x); x += dpp<ROR8>(x); return x;
}
__device__ __forceinline__ float max_row16(float x) {
    x = fmaxf(x, dpp<XOR1>(x)); x = fmaxf(x, dpp<XOR2>(x)); x = fmaxf(x, dpp<XOR7>(x)); x = fmaxf(x, dpp<ROR8>(x)); return x;
}
__device__ __forceinline__ float sum_wave(float x) { return sum32x(sum16x(sum_row16(x))); }
__device__ __forceinline__ float rdlane(float x, int l) { return __builtin_bit_cast(float, __builtin_amdgcn_readlane(__builtin_bit_cast(int, x), l)); }

#define BARRIER() asm volatile("s_waitcnt lgkmcnt(0)\n\ts_barrier" ::: "memory")
#define CBAR() asm volatile("" ::: "memory")
#define SCHED() __builtin_amdgcn_sched_barrier(0)

#define KPOL "nt"
#define VPOL "nt"
#define WPOL "nt"
#define WNT 1
__device__ __forceinline__ v4u ld_nt(const void* p) { return WNT ? __builtin_nontemporal_load((const GAS v4u*)p) : *(const GAS v4u*)p; }
__device__ __forceinline__ v2u ld_nt2(const void* p) { return __builtin_nontemporal_load((const GAS v2u*)p); }
__device__ __forceinline__ unsigned ld_nt1(const void* p) { return __builtin_nontemporal_load((const GAS unsigned*)p); }
__device__ __forceinline__ v4u ld_g(__amdgpu_buffer_rsrc_t r, unsigned off) { return __builtin_amdgcn_raw_buffer_load_b128(r, off, 0, 16); }
__device__ __forceinline__ v2u ld_g2(__amdgpu_buffer_rsrc_t r, unsigned off) { return __builtin_amdgcn_raw_buffer_load_b64(r, off, 0, 16); }
__device__ __forceinline__ void st_g(__amdgpu_buffer_rsrc_t r, unsigned off, v4u v) { __builtin_amdgcn_raw_buffer_store_b128(v, r, off, 0, 16); }
__device__ __forceinline__ void st_g2(__amdgpu_buffer_rsrc_t r, unsigned off, v2u v) { __builtin_amdgcn_raw_buffer_store_b64(v, r, off, 0, 16); }
__device__ __forceinline__ bool tagok(v4u v, unsigned tag) { return v[1] == tag && v[3] == tag; }
__device__ __forceinline__ unsigned lds_ld_vol(unsigned* p) { return __hip_atomic_load(p, __ATOMIC_RELAXED, __HIP_MEMORY_SCOPE_WORKGROUP); }

__device__ __forceinline__ void ln_params(float mean, float var, float& mb, float& inv_den) {
    mb = rbf(mean);
    inv_den = __builtin_amdgcn_rcpf(rbf(__builtin_amdgcn_sqrtf(rbf(rbf(var) + 1e-5f))));
}
__device__ __forceinline__ void tile_load(const u16* p, size_t stride, int lane, v4u& r0, v4u& r1) {
    const u16* q = p + (size_t)(lane >> 2) * stride + (lane & 3) * 8;
    r0 = ld_nt(q); r1 = ld_nt(q + 16 * stride);
}
__device__ __forceinline__ void tile_frags(unsigned* t, int lane, v4u r0, v4u r1, v4u& f0, v4u& f1) {
    const int row = lane >> 2;
    unsigned* w = t + row * 16 + (row >> 3) * 16 + (lane & 3) * 4;
    *(v4u*)w = r0; *(v4u*)(w + 288) = r1;
    CBAR();
    const unsigned* r = t + (lane >> 4) * 144 + (lane & 15);
#pragma unroll
    for (int i = 0; i < 4; ++i) {
        const unsigned a = r[32 * i], c = r[32 * i + 16];
        f0[i] = plo(a, c); f1[i] = phi(a, c);
    }
}
__device__ __forceinline__ void repoll(__amdgpu_buffer_rsrc_t r, unsigned off, v4u& v, bool ok) {
    const v4u t = ld_g(r, ok ? 0xFFFFFF00u : off);
#pragma unroll
    for (int i = 0; i < 4; ++i) v[i] = ok ? v[i] : t[i];
}
#define THR_A 32
#define THR_B 0
#define PSLEEP 1
#define POLL_PAUSE() __builtin_amdgcn_s_sleep(PSLEEP)
#define QPOLL_GAP 8

template <int ROLE>
__device__ __forceinline__ void body(const u16* __restrict__ x, const u16* __restrict__ kc, const u16* __restrict__ vc,
                                     const u16* __restrict__ wq, const u16* __restrict__ wo, const u16* __restrict__ w1,
                                     const u16* __restrict__ w2, u16* __restrict__ out, char* ws, unsigned* lds,
                                     const int kvh, const int j, const int wave, const int lane) {
    const __amdgpu_buffer_rsrc_t rs = __builtin_amdgcn_make_buffer_rsrc(ws, 0, WS_USED, 0x00020000);
    float* ldf = (float*)lds;
    STAMP(0);
    __builtin_amdgcn_s_setprio(3);
    const int b = j >> 4, s = j & 15;
    const int n16 = lane & 15, g = lane >> 4;
    const bool bit3 = (lane & 8) != 0, bit4 = (lane & 16) != 0;
    unsigned* wt = lds + L_WT + wave * (2 * TILE_DW);
    const unsigned epoch = __hip_atomic_load((GAS unsigned*)(ws + E_OFF), __ATOMIC_RELAXED, __HIP_MEMORY_SCOPE_AGENT);
    v4u K[8], V[8];
    v4u W0, W1, W2, W3; unsigned xres = 0;
    unsigned stmp;
    {
        const size_t kvrow = (size_t)(b * 8 + kvh) * SK + s * 256;
        const unsigned ko = ((n16 & 7) * DH + ((n16 >> 3) * 4 + g) * 8) * 2, ko2 = ko + 4096;
        const unsigned vo = lane * 16, vo2 = vo + 4096;
        const u16* kb = kc + (kvrow + wave * 16) * DH;
        const u16* vb = vc + (kvrow + wave * 16) * DH;
#define KV_LOADS \
            "global_load_dwordx4 %0, %[ko], %[kb] " KPOL "\n\tglobal_load_dwordx4 %1, %[ko], %[kb] offset:128 " KPOL "\n\t" \
            "global_load_dwordx4 %2, %[ko], %[kb] offset:256 " KPOL "\n\tglobal_load_dwordx4 %3, %[ko], %[kb] offset:384 " KPOL "\n\t" \
            "global_load_dwordx4 %4, %[ko2], %[kb] " KPOL "\n\tglobal_load_dwordx4 %5, %[ko2], %[kb] offset:128 " KPOL "\n\t" \
            "global_load_dwordx4 %6, %[ko2], %[kb] offset:256 " KPOL "\n\tglobal_load_dwordx4 %7, %[ko2], %[kb] offset:384 " KPOL "\n\t" \
            "global_load_dwordx4 %8, %[vo], %[vb] " VPOL "\n\tglobal_load_dwordx4 %9, %[vo], %[vb] offset:1024 " VPOL "\n\t" \
            "global_load_dwordx4 %10, %[vo], %[vb] offset:2048 " VPOL "\n\tglobal_load_dwordx4 %11, %[vo], %[vb] offset:3072 " VPOL "\n\t" \
            "global_load_dwordx4 %12, %[vo2], %[vb] " VPOL "\n\tglobal_load_dwordx4 %13, %[vo2], %[vb] offset:1024 " VPOL "\n\t" \
            "global_load_dwordx4 %14, %[vo2], %[vb] offset:2048 " VPOL "\n\tglobal_load_dwordx4 %15, %[vo2], %[vb] offset:3072 " VPOL "\n\t"
#define KV_OUT(c) c(K[0]), c(K[1]), c(K[2]), c(K[3]), c(K[4]), c(K[5]), c(K[6]), c(K[7]), c(V[0]), c(V[1]), c(V[2]), c(V[3]), c(V[4]), c(V[5]), c(V[6]), c(V[7])
        v4u xf = {0, 0, 0, 0}, xa = {0, 0, 0, 0};
        if (wave < 8) {
            const int lc = lane < 56 ? lane : 55;
            const u16* p0 = wave ? wq + (size_t)((j >> 4) * 224 + (wave - 1) * 32 + (lane >> 2)) * NQ + kvh * 512 + (j & 15) * 32 + (lane & 3) * 8 : x + lc * 8;
            const u16* p1 = wave ? p0 + (size_t)16 * NQ : p0 + DM;
            asm volatile("s_cmp_eq_u32 %[wv], 0\n\ts_cbranch_scc1 1f\n\t"
                         "global_load_dwordx4 %0, %2, off " WPOL "\n\tglobal_load_dwordx4 %1, %3, off " WPOL "\n\ts_branch 2f\n"
                         "1:\n\tglobal_load_dwordx4 %0, %2, off\n\tglobal_load_dwordx4 %1, %3, off\n"
                         "2:" : "=&v"(xf), "=&v"(xa) : "v"(p0), "v"(p1), [wv] "s"(wave) : "memory", "scc");
        }
        SCHED();
        asm volatile("s_barrier" ::: "memory");
        STAMP(13);
        {
            const int i = (wave - 8) & 7;
            const u16* hk = kc + kvrow * DH + (i >> 2) * 8 * DH + (i & 3) * 64;
            const u16* hv = vc + kvrow * DH + i * 512;
            const unsigned lk = (unsigned)(size_t)(__attribute__((address_space(3))) unsigned*)(lds + L_HK + i * 256);
            unsigned keep;
            asm volatile("s_cmp_lt_u32 %[wv], 8\n\ts_cbranch_scc1 1f\n\t"
                         "s_setprio 1\n\t"
                         "s_mov_b32 %[keep], m0\n\ts_mov_b32 m0, %[lk]\n\ts_nop 0\n\tglobal_load_lds_dwordx4 %[ko], %[hk] " KPOL "\n\t"
                         "s_add_u32 m0, m0, 0x2000\n\ts_nop 0\n\tglobal_load_lds_dwordx4 %[vo], %[hv] " VPOL "\n\ts_mov_b32 m0, %[keep]\n\t"
                         KV_LOADS
                         "1:"
                         : KV_OUT("=&v"), [keep] "=&s"(keep)
                         : [wv] "s"(wave), [ko] "v"(ko), [ko2] "v"(ko2), [vo] "v"(vo), [vo2] "v"(vo2), [kb] "s"(kb), [vb] "s"(vb),
                           [hk] "s"(hk), [hv] "s"(hv), [lk] "s"(lk)
                         : "memory", "scc");
        }
        if (wave < 8) {
            const unsigned tag = epoch + 1u;
            asm volatile("s_waitcnt vmcnt(0)" : "+v"(xf), "+v"(xa));
            if (wave) {
                v4u b0, b1;
                tile_frags(wt, lane, xf, xa, b0, b1);
                asm volatile("" : "+v"(b0), "+v"(b1));
                for (unsigned spins = 0; !__all(lds_ld_vol(lds + L_FL) == tag) && spins < (SPIN_CAP << 6); ++spins) __builtin_amdgcn_s_sleep(1);
                CBAR();
                const v4u a0 = *(const v4u*)(lds + L_XN + (n16 & 1) * (DM / 2) + (j >> 4) * 112 + (wave - 1) * 16 + g * 4);
                const v4f z = {0.f, 0.f, 0.f, 0.f};
                const v4f d0 = mfma(a0, b0, z), d1 = mfma(a0, b1, z);
                if (lane < 16) *(v4f*)(ldf + L_QP + ((wave - 1) * 16 + n16) * 4) = (v4f){d0[0], d1[0], d0[1], d1[1]};
                asm volatile("s_waitcnt lgkmcnt(0)" ::: "memory");
                if (lane == 0) __hip_atomic_store(lds + L_FL + wave, tag, __ATOMIC_RELAXED, __HIP_MEMORY_SCOPE_WORKGROUP);
                STAMP(1);
            } else {
                if (lane < 12) lds[L_ZERO + lane] = (lane >= 4 && lane < 8) ? 0x3f803f80u : 0u;
                const float msk = lane < 56 ? 1.f : 0.f;
                float s0 = 0.f, s1 = 0.f;
#pragma unroll
                for (int k = 0; k < 4; ++k) { s0 += bflo(xf[k]) + bfhi(xf[k]); s1 += bflo(xa[k]) + bfhi(xa[k]); }
                s0 = sum_wave(s0 * msk) * (1.f / DM); s1 = sum_wave(s1 * msk) * (1.f / DM);
                float v0 = 0.f, v1 = 0.f;
#pragma unroll
                for (int k = 0; k < 4; ++k) {
                    float d;
                    d = bflo(xf[k]) - s0; v0 = fmaf(d, d, v0); d = bfhi(xf[k]) - s0; v0 = fmaf(d, d, v0);
                    d = bflo(xa[k]) - s1; v1 = fmaf(d, d, v1); d = bfhi(xa[k]) - s1; v1 = fmaf(d, d, v1);
                }
                v0 = sum_wave(v0 * msk) * (1.f / DM); v1 = sum_wave(v1 * msk) * (1.f / DM);
                float mb0, id0, mb1, id1;
                ln_params(s0, v0, mb0, id0); ln_params(s1, v1, mb1, id1);
                v4u n0, n1;
#pragma unroll
                for (int k = 0; k < 4; ++k) {
                    const unsigned c0 = pkbf(bflo(xf[k]) - mb0, bfhi(xf[k]) - mb0), c1 = pkbf(bflo(xa[k]) - mb1, bfhi(xa[k]) - mb1);
                    n0[k] = pkbf(bflo(c0) * id0, bfhi(c0) * id0);
                    n1[k] = pkbf(bflo(c1) * id1, bfhi(c1) * id1);
                }
                if (lane < 56) { *(v4u*)(lds + L_XN + lane * 4) = n0; *(v4u*)(lds + L_XN + DM / 2 + lane * 4) = n1; }
                asm volatile("s_waitcnt lgkmcnt(0)" ::: "memory");
                if (lane == 0) __hip_atomic_store(lds + L_FL, tag, __ATOMIC_RELAXED, __HIP_MEMORY_SCOPE_WORKGROUP);
                STAMP(12);
                for (unsigned spins = 0; !__all(lds_ld_vol(lds + L_FL + (lane & 7)) == tag) && spins < (SPIN_CAP << 6); ++spins) {}
                CBAR();
                {
                    v2f t0 = {0.f, 0.f};
#pragma unroll
                    for (int w = 0; w < 7; ++w) t0 += *(const v2f*)(ldf + L_QP + (w * 16 + n16) * 4 + (g & 1) * 2);
                    const v4u gq = {__float_as_uint(t0[0]), tag, __float_as_uint(t0[1]), tag};
                    if (lane < 32) st_g(rs, QG_OFF + ((kvh * 32 + j) * 32 + lane) * 16, gq);
                }
                STAMP(1);
                const unsigned base = QG_OFF + ((kvh * 32 + g) * 32 + b * 16 + n16) * 16;
                v4u gg[8];
                __builtin_amdgcn_s_sleep(QPOLL_GAP);
                for (unsigned spins = 0;; ++spins) {
                    CBAR();
                    bool ok = true;
#pragma unroll
                    for (int i = 0; i < 8; ++i) gg[i] = ld_g(rs, base + i * 2048);
#pragma unroll
                    for (int i = 0; i < 8; ++i) ok &= tagok(gg[i], tag);
                    if (__all(ok) || spins > SPIN_CAP) break;
                }
#pragma unroll
                for (int m = 0; m < 4; ++m)
                    lds[L_QS + m * 64 + lane] = pkbf(__uint_as_float(gg[m][0]) + __uint_as_float(gg[4 + m][0]), __uint_as_float(gg[m][2]) + __uint_as_float(gg[4 + m][2]));
            }
            __builtin_amdgcn_s_waitcnt(0x0F70);
        }
        asm volatile("s_sub_u32 %[t], %[wv], 1\n\ts_cmp_gt_u32 %[t], 6\n\ts_cbranch_scc1 1f\n\t"
                     "s_setprio 1\n\t"
                     KV_LOADS
                     "1:\n\ts_setprio 0\n\t"
                     "s_waitcnt vmcnt(16) lgkmcnt(0)\n\ts_barrier\n\t"
                     "s_cmp_lg_u32 %[wv], 0\n\ts_cbranch_scc1 2f\n\t"
                     "ds_read_b128 %0, %[hl]\n\tds_read_b128 %1, %[hl] offset:1024\n\tds_read_b128 %2, %[hl] offset:2048\n\tds_read_b128 %3, %[hl] offset:3072\n\t"
                     "ds_read_b128 %4, %[hl] offset:4096\n\tds_read_b128 %5, %[hl] offset:5120\n\tds_read_b128 %6, %[hl] offset:6144\n\tds_read_b128 %7, %[hl] offset:7168\n\t"
                     "ds_read_b128 %8, %[hl] offset:8192\n\tds_read_b128 %9, %[hl] offset:9216\n\tds_read_b128 %10, %[hl] offset:10240\n\tds_read_b128 %11, %[hl] offset:11264\n\t"
                     "ds_read_b128 %12, %[hl] offset:12288\n\tds_read_b128 %13, %[hl] offset:13312\n\tds_read_b128 %14, %[hl] offset:14336\n\tds_read_b128 %15, %[hl] offset:15360\n\t"
                     "s_waitcnt lgkmcnt(0)\n\t"
                     "2:"
                     : KV_OUT("+v"), [t] "=&s"(stmp)
                     : [wv] "s"(wave), [ko] "v"(ko), [ko2] "v"(ko2), [vo] "v"(vo), [vo2] "v"(vo2), [kb] "s"(kb), [vb] "s"(vb),
                       [hl] "v"((unsigned)(size_t)(__attribute__((address_space(3))) unsigned*)(lds + L_HK) + lane * 16)
                     : "memory", "scc");
        STAMP(2);
    }
    float t;
    {
        const unsigned* qa = lds + L_QS + (n16 & 1) * 128 + (((n16 >> 1) & 1) * 4 + g) * 4;
        v4f d0 = {0.f, 0.f, 0.f, 0.f}, d1 = {0.f, 0.f, 0.f, 0.f};
        v4u qf[4];
#pragma unroll
        for (int i = 0; i < 4; ++i) qf[i] = *(const v4u*)(qa + i * 32);
        asm volatile("s_waitcnt vmcnt(8)" : "+v"(K[0]), "+v"(K[1]), "+v"(K[2]), "+v"(K[3]), "+v"(K[4]), "+v"(K[5]), "+v"(K[6]), "+v"(K[7]));
#pragma unroll
        for (int i = 0; i < 4; ++i) { d0 = mfma(qf[i], K[i], d0); d1 = mfma(qf[i], K[4 + i], d1); }
        const float s0 = trdpp<ROR8>(bit3 ? d0[2] : d0[0], bit3 ? d1[2] : d1[0], bit3);
        const float s1 = trdpp<ROR8>(bit3 ? d0[3] : d0[1], bit3 ? d1[3] : d1[1], bit3);
        t = rbf(bit4 ? s1 : s0) * (0.0625f * 1.4426950408889634f);
    }
    {
        const float mw = max_row16(t);
        if ((lane & 47) == 0) ldf[L_ML + (lane >> 4) * 16 + wave] = mw;
    }
    BARRIER();
    {
        const float M = max_row16(ldf[L_ML + (g & 1) * 16 + n16]);
        const float p = __builtin_amdgcn_exp2f(t - M);
        if (lane < 32) ((u16*)(lds + L_PA + wave * 16))[(g * 2 + (n16 & 1)) * 8 + (n16 >> 1)] = (u16)pkbf(p, p);
        CBAR();
        const float lw = sum_row16(p);
        if ((lane & 47) == 0) { ldf[L_ML + 32 + (lane >> 4) * 16 + wave] = lw; if (wave == 0) ldf[L_MF + (lane >> 4)] = M; }
    }
    v4u PA[4];
#pragma unroll
    for (int tb = 0; tb < 4; ++tb) {
        const bool on = (n16 >> 2) == tb && ((n16 >> 1) & 1) == (g & 1);
        PA[tb] = *(const v4u*)(lds + (on ? L_PA + wave * 16 + ((n16 & 1) * 2 + (g >> 1)) * 4 : L_ZERO));
    }
    STAMP(3);
    {
        v4f acc0 = {0.f, 0.f, 0.f, 0.f}, acc1 = {0.f, 0.f, 0.f, 0.f};
        asm volatile("s_waitcnt vmcnt(0)" : "+v"(V[0]), "+v"(V[1]), "+v"(V[2]), "+v"(V[3]), "+v"(V[4]), "+v"(V[5]), "+v"(V[6]), "+v"(V[7]));
#pragma unroll
        for (int tt = 0; tt < 8; ++tt) {
            v4u bt;
#pragma unroll
            for (int ii = 0; ii < 4; ++ii)
                bt[ii] = (tt & 1) ? phi(V[2 * ii][tt >> 1], V[2 * ii + 1][tt >> 1]) : plo(V[2 * ii][tt >> 1], V[2 * ii + 1][tt >> 1]);
            if (tt < 4) acc0 = mfma(PA[tt & 3], bt, acc0); else acc1 = mfma(PA[tt & 3], bt, acc1);
        }
        *(v4f*)(ldf + L_MB + ((wave * 2 + 0) * 64 + lane) * 4) = acc0;
        *(v4f*)(ldf + L_MB + ((wave * 2 + 1) * 64 + lane) * 4) = acc1;
        asm volatile("s_waitcnt lgkmcnt(0)" ::: "memory");
        if (lane == 0) __hip_atomic_fetch_add(lds + L_MF + 2, 1u, __ATOMIC_RELAXED, __HIP_MEMORY_SCOPE_WORKGROUP);
    }
    STAMP(4);
    unsigned tag = epoch;
    asm volatile("v_add_u32 %0, 1, %0" : "+v"(tag));
    if constexpr (ROLE == 1) {
        if (wave >= 4 && wave < 12) {
            const int h = kvh * 2 + j / 7, cc = j % 7;
            const u16* p = wo + (size_t)(h * 256 + (wave - 4) * 32 + (lane >> 3)) * DM + cc * 64 + (lane & 7) * 8;
            W0 = ld_nt(p); W1 = ld_nt(p + 8 * DM); W2 = ld_nt(p + 16 * DM); W3 = ld_nt(p + 24 * DM);
            unsigned* w = lds + L_WT + ((wave - 4) * 32 + (lane >> 3)) * 32 + (lane & 7) * 4;
            *(v4u*)w = W0; *(v4u*)(w + 8 * 32) = W1; *(v4u*)(w + 16 * 32) = W2; *(v4u*)(w + 24 * 32) = W3;
        }
    }
    SCHED();
    if (wave < 4) {
        __builtin_amdgcn_s_setprio(3);
        for (unsigned spins = 0; lds_ld_vol(lds + L_MF + 2) < 16u && spins < (SPIN_CAP << 8); ++spins) {}
        CBAR();
        const int lam = wave * 16 + n16;
        v4f S0 = {0.f, 0.f, 0.f, 0.f}, S1 = {0.f, 0.f, 0.f, 0.f};
        const float lw = ldf[L_ML + 32 + (g >> 1) * 16 + n16];
        const float M = ldf[L_MF + (g >> 1)];
#pragma unroll
        for (int k = 0; k < 4; ++k) {
            S0 += *(const v4f*)(ldf + L_MB + (((g * 4 + k) * 2 + 0) * 64 + lam) * 4);
            S1 += *(const v4f*)(ldf + L_MB + (((g * 4 + k) * 2 + 1) * 64 + lam) * 4);
        }
        const float L = sum_row16(lw);
        const float inv = __builtin_amdgcn_rcpf(L);
        const float e0 = sum16x(tr32(S0[0], S0[1])) * inv, e1 = sum16x(tr32(S1[0], S1[1])) * inv;
        const float e2 = sum16x(tr32(S0[2], S0[3])) * inv, e3 = sum16x(tr32(S1[2], S1[3])) * inv;
        const int hg = kvh * 2 + (g >> 1);
        if (!bit4) {
            const v4u gv = {pkbf(e0, e1), tag, pkbf(e2, e3), tag};
            st_g(rs, AG_OFF + (((hg * 2 + b) * 16 + s) * 64 + lam) * 16, gv);
        } else if (wave == 0 && n16 == 0) {
            const v2u gs = {__float_as_uint(M + __builtin_amdgcn_logf(L)), tag};
            st_g2(rs, ST_OFF + ((hg * 2 + b) * 16 + s) * 128, gs);
        }
        __builtin_amdgcn_s_setprio(0);
    }
    STAMP(5);
    if constexpr (ROLE == 2) {
        const int f = (j - 14) * 8 + kvh;
        const int wr = wave < 14 ? wave : 13;
        tile_load(w1 + (size_t)(wr * 32) * FF + f * 32, FF, lane, W0, W1);
        tile_load(w2 + (size_t)(f * 32) * DM + wr * 32, DM, lane, W2, W3);
        xres = *(const GAS u16*)(x + ((lane >> 3) & 1) * DM + wr * 32 + (lane & 7) * 4 + g);
        SCHED();
    }
    if constexpr (ROLE == 1) {
        const int h = kvh * 2 + j / 7, cc = j % 7, kk = wave >> 1, bb = wave & 1;
        const unsigned a0 = AG_OFF + ((((h * 2 + bb) * 16 + n16) * 8 + kk) * 8 + 2 * g) * 16;
        const unsigned a2 = ST_OFF + ((h * 2 + bb) * 16 + n16) * 128;
        if (wave == 15) {
            const unsigned af = ST_OFF + (h * 32 + (lane & 31)) * 128;
            for (unsigned spins = 0;; ++spins) {
                CBAR();
                const v2u fl = ld_g2(rs, af);
                if (__builtin_popcountll(__ballot(fl[1] != tag) & 0xffffffffull) <= THR_A || spins > SPIN_CAP) break;
                __builtin_amdgcn_s_sleep(2);
            }
        }
        STAMP(10);
        BARRIER();
        STAMP(11);
        v4u d0 = ld_g(rs, a0), d1 = ld_g(rs, a0 + 16);
        v2u st0 = ld_g2(rs, a2);
        SCHED();
        v4u B[4];
#pragma unroll
        for (int i = 0; i < 4; ++i) {
            const int lam = 8 * kk + 2 * g + (i >> 1), e = (2 * i) & 3;
            const int row = 128 * (e >> 1) + 8 * (lam & 15) + (lam >> 4);
            const v2u r0 = *(const v2u*)(lds + L_WT + row * 32 + 2 * n16), r1 = *(const v2u*)(lds + L_WT + (row + 4) * 32 + 2 * n16);
            B[0][i] = plo(r0[0], r1[0]); B[1][i] = phi(r0[0], r1[0]);
            B[2][i] = plo(r0[1], r1[1]); B[3][i] = phi(r0[1], r1[1]);
        }
        asm volatile("" : "+v"(B[0]), "+v"(B[1]), "+v"(B[2]), "+v"(B[3]));
        for (unsigned spins = 0;; ++spins) {
            const bool ok = tagok(d0, tag) && tagok(d1, tag) && st0[1] == tag;
            if (__all(ok) || spins > SPIN_CAP) break;
            POLL_PAUSE();
            CBAR();
            repoll(rs, a0, d0, ok); repoll(rs, a0 + 16, d1, ok);
            const v2u t2 = ld_g2(rs, ok ? 0xFFFFFF00u : a2);
            st0[0] = ok ? st0[0] : t2[0]; st0[1] = ok ? st0[1] : t2[1];
        }
        STAMP(6);
        const v4u a = {d0[0], d0[2], d1[0], d1[2]};
        const v4f z = {0.f, 0.f, 0.f, 0.f};
        const v4f D0 = mfma(a, B[0], z), D1 = mfma(a, B[1], z), D2 = mfma(a, B[2], z), D3 = mfma(a, B[3], z);
        const float ls = __uint_as_float(st0[0]);
        const float fe = __builtin_amdgcn_exp2f(ls - max_row16(ls));
        const int fi = __float_as_int(fe);
        const float f0 = __int_as_float(__builtin_amdgcn_ds_bpermute(16 * g + 0, fi)), f1 = __int_as_float(__builtin_amdgcn_ds_bpermute(16 * g + 4, fi));
        const float f2 = __int_as_float(__builtin_amdgcn_ds_bpermute(16 * g + 8, fi)), f3 = __int_as_float(__builtin_amdgcn_ds_bpermute(16 * g + 12, fi));
        const float inv = __builtin_amdgcn_rcpf(sum_row16(fe));
        const float y0 = fmaf(f3, D0[3], fmaf(f2, D0[2], fmaf(f1, D0[1], f0 * D0[0])));
        const float y1 = fmaf(f3, D1[3], fmaf(f2, D1[2], fmaf(f1, D1[1], f0 * D1[0])));
        const float y2 = fmaf(f3, D2[3], fmaf(f2, D2[2], fmaf(f1, D2[1], f0 * D2[0])));
        const float y3 = fmaf(f3, D3[3], fmaf(f2, D3[2], fmaf(f1, D3[1], f0 * D3[0])));
        const float y = tr16(tr32(y0, y2), tr32(y1, y3)) * inv;
        ldf[L_RP + wave * 64 + 4 * n16 + g] = y;
        BARRIER();
        if (wave == 0) {
            const int b2 = g & 1;
            v4f acc = {0.f, 0.f, 0.f, 0.f};
#pragma unroll
            for (int k = 0; k < 8; ++k) acc += *(const v4f*)(ldf + L_RP + (2 * k + b2) * 64 + 4 * n16);
            const v4u gv = {pkbf(acc[0], acc[1]), tag, pkbf(acc[2], acc[3]), tag};
            if (lane < 32) st_g(rs, YG_OFF + ((((h * 7 + cc) * 2 + (n16 >> 3)) * 2 + b2) * 8 + (n16 & 7)) * 16, gv);
        }
        STAMP(7);
    } else if constexpr (ROLE == 2) {
        const int f = (j - 14) * 8 + kvh;
        v4u B1[2], B2[2];
        tile_frags(wt, lane, W0, W1, B1[0], B1[1]);
        tile_frags(wt + TILE_DW, lane, W2, W3, B2[0], B2[1]);
        asm volatile("" : "+v"(B1[0]), "+v"(B1[1]), "+v"(B2[0]), "+v"(B2[1]), "+v"(xres));
        const v4f z4 = {0.f, 0.f, 0.f, 0.f};
        unsigned klo = 0x00003f80u, khi = 0x3f800000u;
        asm volatile("" : "+v"(klo), "+v"(khi));
        if (wave == 15) {
            const unsigned af = YG_OFF + ((lane + f) % 112) * 512;
            for (unsigned spins = 0;; ++spins) {
                CBAR();
                const v2u fl = ld_g2(rs, af);
                if (__builtin_popcountll(__ballot(fl[1] == tag)) >= THR_B || spins > SPIN_CAP) break;
                __builtin_amdgcn_s_sleep(2);
            }
        }
        STAMP(10);
        BARRIER();
        STAMP(11);
        if (wave < 14) {
            const unsigned ay = YG_OFF + (((g * 7 + (wave >> 1)) * 2 + (wave & 1)) * 16 + n16) * 16;
            constexpr unsigned HS = 4 * 7 * 512;
            v4u y[4];
#pragma unroll
            for (int i = 0; i < 4; ++i) y[i] = ld_g(rs, ay + i * HS);
            for (unsigned spins = 0;; ++spins) {
                const bool k0 = tagok(y[0], tag), k1 = tagok(y[1], tag), k2 = tagok(y[2], tag), k3 = tagok(y[3], tag);
                if (__all(k0 && k1 && k2 && k3) || spins > SPIN_CAP) break;
                POLL_PAUSE();
                CBAR();
                repoll(rs, ay, y[0], k0); repoll(rs, ay + HS, y[1], k1); repoll(rs, ay + 2 * HS, y[2], k2); repoll(rs, ay + 3 * HS, y[3], k3);
            }
            STAMP(6);
            float c0 = 0.f, c1 = 0.f, c2 = 0.f, c3 = 0.f;
#pragma unroll
            for (int i = 0; i < 4; ++i) {
                c0 = dot2(y[i][0], klo, c0); c1 = dot2(y[i][0], khi, c1);
                c2 = dot2(y[i][2], klo, c2); c3 = dot2(y[i][2], khi, c3);
            }
            const float ys = tr16(tr32(c0, c2), tr32(c1, c3));
            const unsigned hb16 = pkbf(bflo(xres) + rbf(ys), 0.f);
            const float hv = bflo(hb16);
            ((u16*)(lds + L_HB))[(bit3 ? DM : 0) + wave * 32 + (lane & 7) * 4 + g] = (u16)hb16;
            CBAR();
            const v4u av = *(const v4u*)(lds + (n16 == 2 ? L_ONES : L_HB + (n16 & 1) * (DM / 2) + wave * 16 + g * 4));
            const float h0 = bit3 ? rdlane(hv, 8) : rdlane(hv, 0);
            const float dd = hv - h0;
            float sd = dd, sq = dd * dd;
            sd += dpp<XOR1>(sd); sq += dpp<XOR1>(sq); sd += dpp<XOR2>(sd); sq += dpp<XOR2>(sq); sd += dpp<XOR7>(sd); sq += dpp<XOR7>(sq);
            sd = sum16x(sd); sq = sum16x(sq); sd = sum32x(sd); sq = sum32x(sq);
            const v4f D0 = mfma(av, B1[0], z4), D1 = mfma(av, B1[1], z4);
            if ((lane & 55) == 0) *(v4f*)(ldf + L_ST + ((lane >> 3) * 16 + wave) * 4) = (v4f){h0, sd, sq, 0.f};
            if (lane < 16) {
                float* zp = ldf + L_RP + (wave * 32 + n16) * 4;
                *(v4f*)zp = D0; *(v4f*)(zp + 64) = D1;
            }
        }
        BARRIER();
        if (wave != 0) STAMP(12);
        if (wave < 2) {
            const float* zp = ldf + L_RP + (wave * 16 + n16) * 4;
            const v4f sv = *(const v4f*)(ldf + L_ST + ((g >> 1) * 16 + (n16 < 14 ? n16 : 13)) * 4);
            v4f zs = *(const v4f*)(zp + g * 128) + *(const v4f*)(zp + (g + 4) * 128) + *(const v4f*)(zp + (g + 8) * 128);
            const v4f zl = *(const v4f*)(g < 2 ? zp + (g + 12) * 128 : ldf + L_ZERO);
            zs += zl;
            const float valid = n16 < 14 ? 1.f : 0.f;
            const float mw = fmaf(sv[1], 1.f / 32.f, sv[0]);
            const float mean = sum_row16(mw * valid) * (1.f / 14.f);
            const float dm = mw - mean;
            const float m2 = fmaf(32.f * dm, dm, fmaf(-sv[1] * (1.f / 32.f), sv[1], sv[2]));
            const float var = sum_row16(m2 * valid) * (1.f / DM);
            float mb, idn;
            ln_params(mean, fmaxf(var, 0.f), mb, idn);
            const float zb = sum16x(tr32(zs[0], zs[1])), cs = sum16x(sum32x(zs[2]));
            const float zz = rbf((zb - mb * cs) * idn);
            const float act = zz * __builtin_amdgcn_rcpf(1.f + __builtin_amdgcn_exp2f(zz * -1.4426950408889634f));
            if (!bit4) ((u16*)(lds + L_AA))[(g >> 1) * 32 + 2 * n16 + wave] = (u16)pkbf(act, act);
        }
        BARRIER();
        if (wave < 14) {
            __builtin_amdgcn_s_setprio(3);
            const v4u a0 = *(const v4u*)(lds + L_AA + (n16 & 1) * 16 + g * 4);
            const v4f D0 = mfma(a0, B2[0], z4), D1 = mfma(a0, B2[1], z4);
            const int cp = wave * 16 + n16;
            const unsigned o = OG_OFF + ((f * NF + (cp >> 2)) * 8 + (cp & 3)) * 16;
            const v4u g0 = {__float_as_uint(D0[0]), tag, __float_as_uint(D1[0]), tag};
            const v4u g1 = {__float_as_uint(D0[1]), tag, __float_as_uint(D1[1]), tag};
            if (lane < 16) { st_g(rs, o, g0); st_g(rs, o + 64, g1); }
            __builtin_amdgcn_s_setprio(0);
        }
        STAMP(7);
        if (wave == 15) {
            const unsigned a = OG_OFF + (((lane >> 3) * NF + f) * 8 + (lane & 7)) * 16;
            constexpr unsigned FS = 8 * NF * 128;
            const int col = ((lane >> 2) & 1) * DM + f * 8 + 2 * (lane & 3);
            const unsigned hh2 = lds[L_HB + col / 2];
            v4u gv[7];
#pragma unroll
            for (int i = 0; i < 7; ++i) gv[i] = ld_g(rs, a + i * FS);
            for (unsigned spins = 0;; ++spins) {
                bool k[7], ok = true;
#pragma unroll
                for (int i = 0; i < 7; ++i) { k[i] = tagok(gv[i], tag); ok &= k[i]; }
                if (__all(ok) || spins > SPIN_CAP) break;
                CBAR();
#pragma unroll
                for (int i = 0; i < 7; ++i) repoll(rs, a + i * FS, gv[i], k[i]);
            }
            STAMP(8);
            float s0 = 0.f, s1 = 0.f;
#pragma unroll
            for (int i = 0; i < 7; ++i) { s0 += __uint_as_float(gv[i][0]); s1 += __uint_as_float(gv[i][2]); }
            s0 += dpp<ROR8>(s0); s1 += dpp<ROR8>(s1);
            s0 = sum32x(sum16x(s0)); s1 = sum32x(sum16x(s1));
            if (lane < 8) {
                *(GAS unsigned*)(out + col) = pkbf(bflo(hh2) + rbf(s0), bfhi(hh2) + rbf(s1));
                if (f == 0 && lane == 0) __hip_atomic_store((GAS unsigned*)(ws + E_OFF), tag, __ATOMIC_RELAXED, __HIP_MEMORY_SCOPE_AGENT);
            }
        }
        STAMP(9);
    }
}

__global__ void __launch_bounds__(NTHR, 4)
fused_decode(const u16* __restrict__ x, const u16* __restrict__ kc, const u16* __restrict__ vc, const u16* __restrict__ wq,
             const u16* __restrict__ wo, const u16* __restrict__ w1, const u16* __restrict__ w2, u16* __restrict__ out, char* ws) {
    __shared__ __attribute__((aligned(16))) unsigned lds[L_END];
    const int wave = __builtin_amdgcn_readfirstlane(threadIdx.x >> 6), lane = threadIdx.x & 63;
    const int kvh = blockIdx.x & 7, j = blockIdx.x >> 3;
    const int role = j < 14 ? 1 : (j < 21 ? 2 : 0);
    if (role == 1) body<1>(x, kc, vc, wq, wo, w1, w2, out, ws, lds, kvh, j, wave, lane);
    else if (role == 2) body<2>(x, kc, vc, wq, wo, w1, w2, out, ws, lds, kvh, j, wave, lane);
    else body<0>(x, kc, vc, wq, wo, w1, w2, out, ws, lds, kvh, j, wave, lane);
}

extern "C" void kernel_launch(void* const* d_in, const int* in_sizes, int n_in, void* d_out, int out_size, void* d_ws,
                              size_t ws_size, hipStream_t stream) {
    fused_decode<<<256, NTHR, 0, stream>>>((const u16*)d_in[0], (const u16*)d_in[1], (const u16*)d_in[2], (const u16*)d_in[3],
                                           (const u16*)d_in[4], (const u16*)d_in[6], (const u16*)d_in[7], (u16*)d_out, (char*)d_ws);
}
```

```cpp
#include <hip/hip_runtime.h>
#include <hip/hip_bf16.h>
#include <stdint.h>

typedef unsigned short u16;
typedef unsigned v4u __attribute__((ext_vector_type(4)));
typedef unsigned v2u __attribute__((ext_vector_type(2)));
typedef float v4f __attribute__((ext_vector_type(4)));
typedef float v2f __attribute__((ext_vector_type(2)));
typedef __bf16 bf16x2 __attribute__((ext_vector_type(2)));
typedef __bf16 bf16x8 __attribute__((ext_vector_type(8)));
#define GAS __attribute__((address_space(1)))

constexpr int DM = 448, NQ = 4096, SK = 4096, DH = 256, FF = 1792;
constexpr int NTHR = 1024;

constexpr unsigned E_OFF = 0;
constexpr unsigned QG_OFF = 4096;
constexpr unsigned AG_OFF = QG_OFF + 131072;
constexpr unsigned ST_OFF = AG_OFF + 524288;
constexpr unsigned YG_OFF = ST_OFF + 65536;
constexpr unsigned OG_OFF = YG_OFF + 57344;
constexpr unsigned WS_USED = OG_OFF + 100352;
constexpr int NF = 28;
#define DRY 1
constexpr unsigned SPIN_CAP = 1u << 17;

#define STAMP(idx) do {} while (0)

constexpr int L_QS = 0;
constexpr int L_TAG = 256;
constexpr int L_ZERO = 260;
constexpr int L_ONES = 264;
constexpr int L_MF = 268;
constexpr int L_ML = 272;
constexpr int L_PA = L_ML + 64;
constexpr int L_QP = L_PA + 256;
constexpr int L_HB = L_QP + 448;
constexpr int L_ST = L_HB + 448;
constexpr int L_AA = L_ST + 128;
constexpr int L_XN = L_AA + 64;
constexpr int L_RP = L_XN + 448;
constexpr int L_FL = L_RP + 3584;
constexpr int L_FS = L_FL + 16;
constexpr int TILE_DW = 576;
constexpr int L_WT = L_FS + 32;
constexpr int L_MB = L_WT + 16 * 2 * TILE_DW;
constexpr int L_HK = L_MB + 8192;
constexpr int L_W2 = L_MB;
constexpr int L_END = L_MB + 14336;

__device__ __forceinline__ float bflo(unsigned v) { return __uint_as_float(v << 16); }
__device__ __forceinline__ float bfhi(unsigned v) { return __uint_as_float(v & 0xffff0000u); }
__device__ __forceinline__ unsigned pkbf(float lo, float hi) {
    bf16x2 r; r[0] = (__bf16)lo; r[1] = (__bf16)hi; return __builtin_bit_cast(unsigned, r);
}
__device__ __forceinline__ float rbf(float f) { return bflo(pkbf(f, f)); }
__device__ __forceinline__ float dot2(unsigned a, unsigned b, float acc) {
    return __builtin_amdgcn_fdot2_f32_bf16(__builtin_bit_cast(bf16x2, a), __builtin_bit_cast(bf16x2, b), acc, false);
}
__device__ __forceinline__ v4f mfma(v4u a, v4u b, v4f c) {
    return __builtin_amdgcn_mfma_f32_16x16x32_bf16(__builtin_bit_cast(bf16x8, a), __builtin_bit_cast(bf16x8, b), c, 0, 0, 0);
}
__device__ __forceinline__ unsigned plo(unsigned r0, unsigned r1) { return __builtin_amdgcn_perm(r1, r0, 0x05040100u); }
__device__ __forceinline__ unsigned phi(unsigned r0, unsigned r1) { return __builtin_amdgcn_perm(r1, r0, 0x07060302u); }
template <int CTRL> __device__ __forceinline__ float dpp(float x) {
    return __builtin_bit_cast(float, __builtin_amdgcn_mov_dpp(__builtin_bit_cast(int, x), CTRL, 0xf, 0xf, true));
}
constexpr int XOR1 = 0xB1, XOR2 = 0x4E, XOR7 = 0x141, ROR8 = 0x128, ROR4 = 0x124;
__device__ __forceinline__ float tr32(float a, float b) {
    auto s = __builtin_amdgcn_permlane32_swap(__float_as_uint(a), __float_as_uint(b), false, false);
    return __uint_as_float(s[0]) + __uint_as_float(s[1]);
}
__device__ __forceinline__ float tr16(float a, float b) {
    auto s = __builtin_amdgcn_permlane16_swap(__float_as_uint(a), __float_as_uint(b), false, false);
    return __uint_as_float(s[0]) + __uint_as_float(s[1]);
}
template <int CTRL> __device__ __forceinline__ float trdpp(float a, float b, bool bit) {
    const float keep = bit ? b : a, send = bit ? a : b;
    return keep + dpp<CTRL>(send);
}
__device__ __forceinline__ float sum32x(float x) { return tr32(x, x); }
__device__ __forceinline__ float sum16x(float x) { return tr16(x, x); }
__device__ __forceinline__ float max32x(float x) {
    auto s = __builtin_amdgcn_permlane32_swap(__float_as_uint(x), __float_as_uint(x), false, false);
    return fmaxf(__uint_as_float(s[0]), __uint_as_float(s[1]));
}
__device__ __forceinline__ float max16x(float x) {
    auto s = __builtin_amdgcn_permlane16_swap(__float_as_uint(x), __float_as_uint(x), false, false);
    return fmaxf(__uint_as_float(s[0]), __uint_as_float(s[1]));
}
__device__ __forceinline__ float sum_row16(float x) {
    x += dpp<XOR1>(x); x += dpp<XOR2>(x); x += dpp<XOR7>(x); x += dpp<ROR8>(x); return x;
}
__device__ __forceinline__ float max_row16(float x) {
    x = fmaxf(x, dpp<XOR1>(x)); x = fmaxf(x, dpp<XOR2>(x)); x = fmaxf(x, dpp<XOR7>(x)); x = fmaxf(x, dpp<ROR8>(x)); return x;
}
__device__ __forceinline__ float sum_wave(float x) { return sum32x(sum16x(sum_row16(x))); }
__device__ __forceinline__ float rdlane(float x, int l) { return __builtin_bit_cast(float, __builtin_amdgcn_readlane(__builtin_bit_cast(int, x), l)); }

#define BARRIER() asm volatile("s_waitcnt lgkmcnt(0)\n\ts_barrier" ::: "memory")
#define CBAR() asm volatile("" ::: "memory")
#define SCHED() __builtin_amdgcn_sched_barrier(0)

#define KPOL "nt"
#define VPOL "nt"
#define WPOL "nt"
#define WNT 1
__device__ __forceinline__ v4u ld_nt(const void* p) { return WNT ? __builtin_nontemporal_load((const GAS v4u*)p) : *(const GAS v4u*)p; }
__device__ __forceinline__ v2u ld_nt2(const void* p) { return __builtin_nontemporal_load((const GAS v2u*)p); }
__device__ __forceinline__ unsigned ld_nt1(const void* p) { return __builtin_nontemporal_load((const GAS unsigned*)p); }
__device__ __forceinline__ v4u ld_g(__amdgpu_buffer_rsrc_t r, unsigned off) { return __builtin_amdgcn_raw_buffer_load_b128(r, off, 0, 16); }
__device__ __forceinline__ v2u ld_g2(__amdgpu_buffer_rsrc_t r, unsigned off) { return __builtin_amdgcn_raw_buffer_load_b64(r, off, 0, 16); }
__device__ __forceinline__ void st_g(__amdgpu_buffer_rsrc_t r, unsigned off, v4u v) { __builtin_amdgcn_raw_buffer_store_b128(v, r, off, 0, 16); }
__device__ __forceinline__ void st_g2(__amdgpu_buffer_rsrc_t r, unsigned off, v2u v) { __builtin_amdgcn_raw_buffer_store_b64(v, r, off, 0, 16); }
__device__ __forceinline__ bool tagok(v4u v, unsigned tag) { return v[1] == tag && v[3] == tag; }
__device__ __forceinline__ unsigned lds_ld_vol(unsigned* p) { return __hip_atomic_load(p, __ATOMIC_RELAXED, __HIP_MEMORY_SCOPE_WORKGROUP); }

__device__ __forceinline__ void ln_params(float mean, float var, float& mb, float& inv_den) {
    mb = rbf(mean);
    inv_den = __builtin_amdgcn_rcpf(rbf(__builtin_amdgcn_sqrtf(rbf(rbf(var) + 1e-5f))));
}
__device__ __forceinline__ void tile_load(const u16* p, size_t stride, int lane, v4u& r0, v4u& r1) {
    const u16* q = p + (size_t)(lane >> 2) * stride + (lane & 3) * 8;
    r0 = ld_nt(q); r1 = ld_nt(q + 16 * stride);
}
__device__ __forceinline__ void tile_frags(unsigned* t, int lane, v4u r0, v4u r1, v4u& f0, v4u& f1) {
    const int row = lane >> 2;
    unsigned* w = t + row * 16 + (row >> 3) * 16 + (lane & 3) * 4;
    *(v4u*)w = r0; *(v4u*)(w + 288) = r1;
    CBAR();
    const unsigned* r = t + (lane >> 4) * 144 + (lane & 15);
#pragma unroll
    for (int i = 0; i < 4; ++i) {
        const unsigned a = r[32 * i], c = r[32 * i + 16];
        f0[i] = plo(a, c); f1[i] = phi(a, c);
    }
}
__device__ __forceinline__ void repoll(__amdgpu_buffer_rsrc_t r, unsigned off, v4u& v, bool ok) {
    const v4u t = ld_g(r, ok ? 0xFFFFFF00u : off);
#pragma unroll
    for (int i = 0; i < 4; ++i) v[i] = ok ? v[i] : t[i];
}
#define NFIN 1
#define FSTAG 4
#define THR_A 32
#define THR_B 0
#define PSLEEP 1
#define POLL_PAUSE() __builtin_amdgcn_s_sleep(PSLEEP)
#define QPOLL_GAP 8

template <int ROLE>
__device__ __forceinline__ void body(const u16* __restrict__ x, const u16* __restrict__ kc, const u16* __restrict__ vc,
                                     const u16* __restrict__ wq, const u16* __restrict__ wo, const u16* __restrict__ w1,
                                     const u16* __restrict__ w2, u16* __restrict__ out, char* ws, unsigned* lds,
                                     const int kvh, const int j, const int wave, const int lane) {
    const __amdgpu_buffer_rsrc_t rs = __builtin_amdgcn_make_buffer_rsrc(ws, 0, WS_USED, 0x00020000);
    float* ldf = (float*)lds;
    STAMP(0);
    __builtin_amdgcn_s_setprio(3);
    const int b = j >> 4, s = j & 15;
    const int n16 = lane & 15, g = lane >> 4;
    const bool bit3 = (lane & 8) != 0, bit4 = (lane & 16) != 0;
    unsigned* wt = lds + L_WT + wave * (2 * TILE_DW);
    unsigned epoch = 0;
    v4u K[8], V[8];
    v4u W0 = {0, 0, 0, 0}, W1 = W0, W2 = W0, W3 = W0; unsigned xres = 0;
    unsigned stmp; unsigned tdummy = 0;
    {
        const size_t kvrow = (size_t)(b * 8 + kvh) * SK + s * 256;
        const unsigned ko = ((n16 & 7) * DH + ((n16 >> 3) * 4 + g) * 8) * 2, ko2 = ko + 4096;
        const unsigned vo = lane * 16, vo2 = vo + 4096;
        const u16* kb = kc + (kvrow + wave * 16) * DH;
        const u16* vb = vc + (kvrow + wave * 16) * DH;
#define KV_LOADS_A \
            "global_load_dwordx4 %0, %[ko], %[kb] " KPOL "\n\tglobal_load_dwordx4 %1, %[ko], %[kb] offset:128 " KPOL "\n\t" \
            "global_load_dwordx4 %2, %[ko], %[kb] offset:256 " KPOL "\n\tglobal_load_dwordx4 %3, %[ko], %[kb] offset:384 " KPOL "\n\t"
#define KV_LOADS_B \
            "global_load_dwordx4 %4, %[ko2], %[kb] " KPOL "\n\tglobal_load_dwordx4 %5, %[ko2], %[kb] offset:128 " KPOL "\n\t" \
            "global_load_dwordx4 %6, %[ko2], %[kb] offset:256 " KPOL "\n\tglobal_load_dwordx4 %7, %[ko2], %[kb] offset:384 " KPOL "\n\t" \
            "global_load_dwordx4 %8, %[vo], %[vb] " VPOL "\n\tglobal_load_dwordx4 %9, %[vo], %[vb] offset:1024 " VPOL "\n\t" \
            "global_load_dwordx4 %10, %[vo], %[vb] offset:2048 " VPOL "\n\tglobal_load_dwordx4 %11, %[vo], %[vb] offset:3072 " VPOL "\n\t" \
            "global_load_dwordx4 %12, %[vo2], %[vb] " VPOL "\n\tglobal_load_dwordx4 %13, %[vo2], %[vb] offset:1024 " VPOL "\n\t" \
            "global_load_dwordx4 %14, %[vo2], %[vb] offset:2048 " VPOL "\n\tglobal_load_dwordx4 %15, %[vo2], %[vb] offset:3072 " VPOL "\n\t"
#define KV_LOADS KV_LOADS_A KV_LOADS_B
#define KV_OUT(c) c(K[0]), c(K[1]), c(K[2]), c(K[3]), c(K[4]), c(K[5]), c(K[6]), c(K[7]), c(V[0]), c(V[1]), c(V[2]), c(V[3]), c(V[4]), c(V[5]), c(V[6]), c(V[7])
        {
            const int i = (wave - 8) & 7;
            const u16* hk = kc + kvrow * DH + (i >> 2) * 8 * DH + (i & 3) * 64;
            const u16* hv = vc + kvrow * DH + i * 512;
            const unsigned lk = (unsigned)(size_t)(__attribute__((address_space(3))) unsigned*)(lds + L_HK + i * 256);
            unsigned keep;
            asm volatile("s_cmp_lt_u32 %[wv], 8\n\ts_cbranch_scc1 1f\n\t"
                         "s_setprio 1\n\t"
                         "s_mov_b32 %[keep], m0\n\ts_mov_b32 m0, %[lk]\n\ts_nop 0\n\tglobal_load_lds_dwordx4 %[ko], %[hk] " KPOL "\n\t"
                         "s_add_u32 m0, m0, 0x2000\n\ts_nop 0\n\tglobal_load_lds_dwordx4 %[vo], %[hv] " VPOL "\n\ts_mov_b32 m0, %[keep]\n\t"
                         KV_LOADS_A
                         "1:"
                         : KV_OUT("=&v"), [keep] "=&s"(keep)
                         : [wv] "s"(wave), [ko] "v"(ko), [ko2] "v"(ko2), [vo] "v"(vo), [vo2] "v"(vo2), [kb] "s"(kb), [vb] "s"(vb),
                           [hk] "s"(hk), [hv] "s"(hv), [lk] "s"(lk)
                         : "memory", "scc");
        }
        v4u xf = {0, 0, 0, 0}, xa = {0, 0, 0, 0};
        if (wave < 8) {
            const int lc = lane < 56 ? lane : 55;
            const u16* p0 = wave ? wq + (size_t)((j >> 4) * 224 + (wave - 1) * 32 + (lane >> 2)) * NQ + kvh * 512 + (j & 15) * 32 + (lane & 3) * 8 : x + lc * 8;
            const u16* p1 = wave ? p0 + (size_t)16 * NQ : p0 + DM;
            const u16* tp = kb;
            tp = (lane & 7) == 1 ? vb : tp;
            tp = (lane & 7) == 2 ? out : tp;
            if constexpr (ROLE == 1) tp = (lane & 7) == 3 ? wo + (size_t)((kvh * 2 + j / 7) * 256 + (lane >> 3) * 32) * DM + (j % 7) * 64 : tp;
            if constexpr (ROLE == 2) {
                tp = (lane & 7) == 3 ? w1 + (size_t)((lane >> 3) * 56) * FF + ((j - 14) * 8 + kvh) * 64 : tp;
                tp = (lane & 7) == 4 ? w2 + (size_t)(((j - 14) * 8 + kvh) * 64 + (lane >> 3) * 8) * DM : tp;
            }
            unsigned dummy;
            asm volatile("s_cmp_eq_u32 %[wv], 0\n\ts_cbranch_scc1 1f\n\t"
                         "global_load_dwordx4 %0, %4, off " WPOL "\n\tglobal_load_dwordx4 %1, %5, off " WPOL "\n\ts_branch 2f\n"
                         "1:\n\tglobal_load_dwordx4 %0, %4, off\n\tglobal_load_dwordx4 %1, %5, off\n\tglobal_load_ushort %2, %6, off\n"
                         "2:\n\tglobal_load_dword %3, %[zero], %[wsb] sc1"
                         : "=&v"(xf), "=&v"(xa), "=&v"(dummy), "+v"(epoch) : "v"(p0), "v"(p1), "v"(tp), [wv] "s"(wave), [zero] "v"(0u), [wsb] "s"(ws) : "memory", "scc");
            tdummy = dummy;
            if (wave == 0) {
                if (lane < 12) lds[L_ZERO + lane] = (lane >= 4 && lane < 8) ? 0x3f803f80u : ((lane == 8 || lane == 9) ? 0xff800000u : 0u);
                if (lane < 16) lds[L_FL + lane] = 0u;
            }
        }
        SCHED();
        asm volatile("s_waitcnt lgkmcnt(0)\n\ts_barrier" ::: "memory");
        STAMP(13);
        asm volatile("s_cmp_lt_u32 %[wv], 8\n\ts_cbranch_scc1 1f\n\t"
                     "global_load_dword %[ep], %[zero], %[wsb] sc1\n\t"
                     KV_LOADS_B
                     "1:"
                     : KV_OUT("+v"), [ep] "+v"(epoch)
                     : [wv] "s"(wave), [ko2] "v"(ko2), [vo] "v"(vo), [vo2] "v"(vo2), [kb] "s"(kb), [vb] "s"(vb), [zero] "v"(0u), [wsb] "s"(ws)
                     : "memory", "scc");
        if (wave < 8) {
            asm volatile("s_waitcnt vmcnt(0)" : "+v"(xf), "+v"(xa), "+v"(tdummy), "+v"(epoch));
            const unsigned tag = epoch + 1u;
            if (wave) {
                v4u b0, b1;
                tile_frags(wt, lane, xf, xa, b0, b1);
                asm volatile("" : "+v"(b0), "+v"(b1));
                for (unsigned spins = 0; lds_ld_vol(lds + L_FL) == 0u && spins < (SPIN_CAP << 6); ++spins) __builtin_amdgcn_s_sleep(1);
                CBAR();
                const v4u a0 = *(const v4u*)(lds + L_XN + (n16 & 1) * (DM / 2) + (j >> 4) * 112 + (wave - 1) * 16 + g * 4);
                const v4f z = {0.f, 0.f, 0.f, 0.f};
                const v4f d0 = mfma(a0, b0, z), d1 = mfma(a0, b1, z);
                if (lane < 16) *(v4f*)(ldf + L_QP + ((wave - 1) * 16 + n16) * 4) = (v4f){d0[0], d1[0], d0[1], d1[1]};
                asm volatile("s_waitcnt lgkmcnt(0)" ::: "memory");
                if (lane == 0) __hip_atomic_fetch_add(lds + L_FL + 1, 1u, __ATOMIC_RELAXED, __HIP_MEMORY_SCOPE_WORKGROUP);
                STAMP(1);
            } else {
                const float msk = lane < 56 ? 1.f : 0.f;
                float s0 = 0.f, s1 = 0.f;
#pragma unroll
                for (int k = 0; k < 4; ++k) { s0 += bflo(xf[k]) + bfhi(xf[k]); s1 += bflo(xa[k]) + bfhi(xa[k]); }
                s0 = sum_wave(s0 * msk) * (1.f / DM); s1 = sum_wave(s1 * msk) * (1.f / DM);
                float v0 = 0.f, v1 = 0.f;
#pragma unroll
                for (int k = 0; k < 4; ++k) {
                    float d;
                    d = bflo(xf[k]) - s0; v0 = fmaf(d, d, v0); d = bfhi(xf[k]) - s0; v0 = fmaf(d, d, v0);
                    d = bflo(xa[k]) - s1; v1 = fmaf(d, d, v1); d = bfhi(xa[k]) - s1; v1 = fmaf(d, d, v1);
                }
                v0 = sum_wave(v0 * msk) * (1.f / DM); v1 = sum_wave(v1 * msk) * (1.f / DM);
                float mb0, id0, mb1, id1;
                ln_params(s0, v0, mb0, id0); ln_params(s1, v1, mb1, id1);
                v4u n0, n1;
#pragma unroll
                for (int k = 0; k < 4; ++k) {
                    const unsigned c0 = pkbf(bflo(xf[k]) - mb0, bfhi(xf[k]) - mb0), c1 = pkbf(bflo(xa[k]) - mb1, bfhi(xa[k]) - mb1);
                    n0[k] = pkbf(bflo(c0) * id0, bfhi(c0) * id0);
                    n1[k] = pkbf(bflo(c1) * id1, bfhi(c1) * id1);
                }
                if (lane < 56) { *(v4u*)(lds + L_XN + lane * 4) = n0; *(v4u*)(lds + L_XN + DM / 2 + lane * 4) = n1; }
                asm volatile("s_waitcnt lgkmcnt(0)" ::: "memory");
                if (lane == 0) __hip_atomic_store(lds + L_FL, 1u, __ATOMIC_RELAXED, __HIP_MEMORY_SCOPE_WORKGROUP);
                STAMP(12);
                for (unsigned spins = 0; lds_ld_vol(lds + L_FL + 1) < 7u && spins < (SPIN_CAP << 6); ++spins) {}
                CBAR();
                {
                    v2f t0 = {0.f, 0.f};
#pragma unroll
                    for (int w = 0; w < 7; ++w) t0 += *(const v2f*)(ldf + L_QP + (w * 16 + n16) * 4 + (g & 1) * 2);
                    const v4u gq = {__float_as_uint(t0[0]), tag, __float_as_uint(t0[1]), tag};
                    if (lane < 32) st_g(rs, QG_OFF + ((kvh * 32 + j) * 32 + lane) * 16, gq);
                }
                STAMP(1);
                const unsigned base = QG_OFF + ((kvh * 32 + g) * 32 + b * 16 + n16) * 16;
                v4u gg[8];
                __builtin_amdgcn_s_sleep(QPOLL_GAP);
                for (unsigned spins = 0;; ++spins) {
                    CBAR();
                    bool ok = true;
#pragma unroll
                    for (int i = 0; i < 8; ++i) gg[i] = ld_g(rs, base + i * 2048);
#pragma unroll
                    for (int i = 0; i < 8; ++i) ok &= tagok(gg[i], tag);
                    if (__all(ok) || spins > SPIN_CAP) break;
                }
#pragma unroll
                for (int m = 0; m < 4; ++m)
                    lds[L_QS + m * 64 + lane] = pkbf(__uint_as_float(gg[m][0]) + __uint_as_float(gg[4 + m][0]), __uint_as_float(gg[m][2]) + __uint_as_float(gg[4 + m][2]));
                asm volatile("s_waitcnt lgkmcnt(0)" ::: "memory");
                if (lane == 0) __hip_atomic_store(lds + L_FL + 2, 1u, __ATOMIC_RELAXED, __HIP_MEMORY_SCOPE_WORKGROUP);
                for (unsigned spins = 0; lds_ld_vol(lds + L_FL + 3) < 8u && spins < (SPIN_CAP << 6); ++spins) __builtin_amdgcn_s_sleep(2);
            }
            __builtin_amdgcn_s_waitcnt(0x0F70);
        }
        asm volatile("s_sub_u32 %[t], %[wv], 1\n\ts_cmp_gt_u32 %[t], 6\n\ts_cbranch_scc1 1f\n\t"
                     "s_setprio 1\n\t"
                     KV_LOADS
                     "1:\n\ts_setprio 0\n\t"
                     "s_cmp_lg_u32 %[wv], 0\n\ts_cbranch_scc1 2f\n\t"
                     "ds_read_b128 %0, %[hl]\n\tds_read_b128 %1, %[hl] offset:1024\n\tds_read_b128 %2, %[hl] offset:2048\n\tds_read_b128 %3, %[hl] offset:3072\n\t"
                     "ds_read_b128 %4, %[hl] offset:4096\n\tds_read_b128 %5, %[hl] offset:5120\n\tds_read_b128 %6, %[hl] offset:6144\n\tds_read_b128 %7, %[hl] offset:7168\n\t"
                     "ds_read_b128 %8, %[hl] offset:8192\n\tds_read_b128 %9, %[hl] offset:9216\n\tds_read_b128 %10, %[hl] offset:10240\n\tds_read_b128 %11, %[hl] offset:11264\n\t"
                     "ds_read_b128 %12, %[hl] offset:12288\n\tds_read_b128 %13, %[hl] offset:13312\n\tds_read_b128 %14, %[hl] offset:14336\n\tds_read_b128 %15, %[hl] offset:15360\n\t"
                     "s_waitcnt lgkmcnt(0)\n\t"
                     "2:"
                     : KV_OUT("+v"), [t] "=&s"(stmp)
                     : [wv] "s"(wave), [ko] "v"(ko), [ko2] "v"(ko2), [vo] "v"(vo), [vo2] "v"(vo2), [kb] "s"(kb), [vb] "s"(vb),
                       [hl] "v"((unsigned)(size_t)(__attribute__((address_space(3))) unsigned*)(lds + L_HK) + lane * 16)
                     : "memory", "scc");
        if (wave >= 8) {
            asm volatile("s_waitcnt vmcnt(17)" ::: "memory");
            if (lane == 0) __hip_atomic_fetch_add(lds + L_FL + 3, 1u, __ATOMIC_RELAXED, __HIP_MEMORY_SCOPE_WORKGROUP);
        }
        for (unsigned spins = 0; lds_ld_vol(lds + L_FL + 2) == 0u && spins < (SPIN_CAP << 6); ++spins) __builtin_amdgcn_s_sleep(2);
        CBAR();
        STAMP(2);
    }
    float t;
    {
        const unsigned* qa = lds + L_QS + (n16 & 1) * 128 + (((n16 >> 1) & 1) * 4 + g) * 4;
        v4f d0 = {0.f, 0.f, 0.f, 0.f}, d1 = {0.f, 0.f, 0.f, 0.f};
        v4u qf[4];
#pragma unroll
        for (int i = 0; i < 4; ++i) qf[i] = *(const v4u*)(qa + i * 32);
        asm volatile("s_waitcnt vmcnt(8)" : "+v"(K[0]), "+v"(K[1]), "+v"(K[2]), "+v"(K[3]), "+v"(K[4]), "+v"(K[5]), "+v"(K[6]), "+v"(K[7]));
#pragma unroll
        for (int i = 0; i < 4; ++i) { d0 = mfma(qf[i], K[i], d0); d1 = mfma(qf[i], K[4 + i], d1); }
        const float s0 = trdpp<ROR8>(bit3 ? d0[2] : d0[0], bit3 ? d1[2] : d1[0], bit3);
        const float s1 = trdpp<ROR8>(bit3 ? d0[3] : d0[1], bit3 ? d1[3] : d1[1], bit3);
        t = rbf(bit4 ? s1 : s0) * (0.0625f * 1.4426950408889634f);
    }
    {
        const float mw = max_row16(t);
        const float p = __builtin_amdgcn_exp2f(t - mw);
        if (lane < 32) ((u16*)(lds + L_PA + wave * 16))[(g * 2 + (n16 & 1)) * 8 + (n16 >> 1)] = (u16)pkbf(p, p);
        CBAR();
        const float lw = sum_row16(p);
        if ((lane & 47) == 0) {
            ldf[L_ML + (lane >> 4) * 16 + wave] = mw; ldf[L_ML + 32 + (lane >> 4) * 16 + wave] = lw;
            __hip_atomic_fetch_max(ldf + L_MF + (lane >> 4), mw, __ATOMIC_RELAXED, __HIP_MEMORY_SCOPE_WORKGROUP);
        }
    }
    v4u PA[4];
#pragma unroll
    for (int tb = 0; tb < 4; ++tb) {
        const bool on = (n16 >> 2) == tb && ((n16 >> 1) & 1) == (g & 1);
        PA[tb] = *(const v4u*)(lds + (on ? L_PA + wave * 16 + ((n16 & 1) * 2 + (g >> 1)) * 4 : L_ZERO));
    }
    STAMP(3);
    {
        v4f acc0 = {0.f, 0.f, 0.f, 0.f}, acc1 = {0.f, 0.f, 0.f, 0.f};
        asm volatile("s_waitcnt vmcnt(0)" : "+v"(V[0]), "+v"(V[1]), "+v"(V[2]), "+v"(V[3]), "+v"(V[4]), "+v"(V[5]), "+v"(V[6]), "+v"(V[7]), "+v"(epoch));
#pragma unroll
        for (int tt = 0; tt < 8; ++tt) {
            v4u bt;
#pragma unroll
            for (int ii = 0; ii < 4; ++ii)
                bt[ii] = (tt & 1) ? phi(V[2 * ii][tt >> 1], V[2 * ii + 1][tt >> 1]) : plo(V[2 * ii][tt >> 1], V[2 * ii + 1][tt >> 1]);
            if (tt < 4) acc0 = mfma(PA[tt & 3], bt, acc0); else acc1 = mfma(PA[tt & 3], bt, acc1);
        }
        *(v4f*)(ldf + L_MB + ((wave * 2 + 0) * 64 + lane) * 4) = acc0;
        *(v4f*)(ldf + L_MB + ((wave * 2 + 1) * 64 + lane) * 4) = acc1;
        asm volatile("s_waitcnt lgkmcnt(0)" ::: "memory");
        if (lane == 0) __hip_atomic_fetch_add(lds + L_MF + 2, 1u, __ATOMIC_RELAXED, __HIP_MEMORY_SCOPE_WORKGROUP);
    }
    STAMP(4);
    const unsigned tag = epoch + 1u;
    if constexpr (ROLE == 1) {
        if (wave >= 4 && wave < 12) {
            const int h = kvh * 2 + j / 7, cc = j % 7;
            const u16* p = wo + (size_t)(h * 256 + (wave - 4) * 32 + (lane >> 3)) * DM + cc * 64 + (lane & 7) * 8;
            W0 = ld_nt(p); W1 = ld_nt(p + 8 * DM); W2 = ld_nt(p + 16 * DM); W3 = ld_nt(p + 24 * DM);
        }
    }
    SCHED();
    if (wave < 4) {
        __builtin_amdgcn_s_setprio(3);
        const int lam = wave * 16 + n16;
        unsigned opub = !bit4 ? AG_OFF + ((((kvh * 2 + (g >> 1)) * 2 + b) * 16 + s) * 64 + lam) * 16 : ST_OFF + (((kvh * 2 + (g >> 1)) * 2 + b) * 16 + s) * 128;
        asm volatile("" : "+v"(opub));
        for (unsigned spins = 0; lds_ld_vol(lds + L_MF + 2) < 16u && spins < (SPIN_CAP << 8); ++spins) {}
        CBAR();
        const v2f MM = *(const v2f*)(ldf + L_MF);
        const float mo = ldf[L_ML + (g >> 1) * 16 + n16];
        const float lw = ldf[L_ML + 32 + (g >> 1) * 16 + n16];
        const v4f m0 = *(const v4f*)(ldf + L_ML + 4 * g), m1 = *(const v4f*)(ldf + L_ML + 16 + 4 * g);
        v4f X0[4], X1[4];
#pragma unroll
        for (int k = 0; k < 4; ++k) {
            X0[k] = *(const v4f*)(ldf + L_MB + (((g * 4 + k) * 2 + 0) * 64 + lam) * 4);
            X1[k] = *(const v4f*)(ldf + L_MB + (((g * 4 + k) * 2 + 1) * 64 + lam) * 4);
        }
        const float M0 = MM[0], M1 = MM[1];
        const float M = (g >> 1) ? M1 : M0;
        v4f S0 = {0.f, 0.f, 0.f, 0.f}, S1 = {0.f, 0.f, 0.f, 0.f};
#pragma unroll
        for (int k = 0; k < 4; ++k) {
            const float f0 = __builtin_amdgcn_exp2f(m0[k] - M0), f1 = __builtin_amdgcn_exp2f(m1[k] - M1);
            S0[0] = fmaf(f0, X0[k][0], S0[0]); S0[1] = fmaf(f1, X0[k][1], S0[1]); S0[2] = fmaf(f0, X0[k][2], S0[2]); S0[3] = fmaf(f1, X0[k][3], S0[3]);
            S1[0] = fmaf(f0, X1[k][0], S1[0]); S1[1] = fmaf(f1, X1[k][1], S1[1]); S1[2] = fmaf(f0, X1[k][2], S1[2]); S1[3] = fmaf(f1, X1[k][3], S1[3]);
        }
        const float L = sum_row16(lw * __builtin_amdgcn_exp2f(mo - M));
        const float inv = __builtin_amdgcn_rcpf(L);
        const float e0 = sum16x(tr32(S0[0], S0[1])) * inv, e1 = sum16x(tr32(S1[0], S1[1])) * inv;
        const float e2 = sum16x(tr32(S0[2], S0[3])) * inv, e3 = sum16x(tr32(S1[2], S1[3])) * inv;
        if (!bit4) {
            const v4u gv = {pkbf(e0, e1), tag, pkbf(e2, e3), tag};
            st_g(rs, opub, gv);
        } else if (wave == 0 && n16 == 0) {
            const v2u gs = {__float_as_uint(M + __builtin_amdgcn_logf(L)), tag};
            st_g2(rs, opub, gs);
        }
        __builtin_amdgcn_s_setprio(0);
    }
    STAMP(5);
    v4u W4 = W0, W5 = W0, W6 = W0, W7 = W0;
    if constexpr (ROLE == 2) {
        const int f = (j - 14) * 8 + kvh;
        const int wr = wave < 14 ? wave : 13;
        for (unsigned spins = 0; lds_ld_vol(lds + L_MF + 2) < 16u && spins < (SPIN_CAP << 8); ++spins) __builtin_amdgcn_s_sleep(2);
        CBAR();
        {
            const u16* p = w1 + (size_t)(wr * 32 + (lane >> 3)) * FF + f * 64 + (lane & 7) * 8;
            W0 = ld_nt(p); W1 = ld_nt(p + 8 * FF); W2 = ld_nt(p + 16 * FF); W3 = ld_nt(p + 24 * FF);
            const u16* q = w2 + (size_t)(f * 64) * DM + wr * 2048 + lane * 8;
            W4 = ld_nt(q); W5 = ld_nt(q + 512); W6 = ld_nt(q + 1024); W7 = ld_nt(q + 1536);
        }
        xres = *(const GAS u16*)(x + ((lane >> 3) & 1) * DM + wr * 32 + (lane & 7) * 4 + g);
        SCHED();
    }
    if constexpr (ROLE == 1) {
        const int h = kvh * 2 + j / 7, cc = j % 7, kk = wave >> 1, bb = wave & 1;
        const unsigned a0 = AG_OFF + ((((h * 2 + bb) * 16 + n16) * 8 + kk) * 8 + 2 * g) * 16;
        const unsigned af = ST_OFF + (h * 32 + (lane & 31)) * 128;
        if (THR_A < 32 && wave == 15) {
            for (unsigned spins = 0;; ++spins) {
                CBAR();
                const v2u fl = ld_g2(rs, af);
                if (__builtin_popcountll(__ballot(fl[1] != tag) & 0xffffffffull) <= THR_A || spins > SPIN_CAP) break;
                __builtin_amdgcn_s_sleep(2);
            }
        }
        STAMP(10);
        BARRIER();
        v4u d0 = ld_g(rs, a0), d1 = ld_g(rs, a0 + 16);
        v2u st0 = ld_g2(rs, wave == 15 ? af : 0xFFFFFF00u);
        SCHED();
        if (wave >= 4 && wave < 12) {
            unsigned* w = lds + L_WT + ((wave - 4) * 32 + (lane >> 3)) * 32 + (lane & 7) * 4;
            *(v4u*)w = W0; *(v4u*)(w + 8 * 32) = W1; *(v4u*)(w + 16 * 32) = W2; *(v4u*)(w + 24 * 32) = W3;
            asm volatile("s_waitcnt lgkmcnt(0)" ::: "memory");
            if (lane == 0) __hip_atomic_fetch_add(lds + L_FL + 5, 1u, __ATOMIC_RELAXED, __HIP_MEMORY_SCOPE_WORKGROUP);
        }
        for (unsigned spins = 0; lds_ld_vol(lds + L_FL + 5) < 8u && spins < (SPIN_CAP << 8); ++spins) __builtin_amdgcn_s_sleep(1);
        CBAR();
        STAMP(11);
        v4u B[4];
#pragma unroll
        for (int i = 0; i < 4; ++i) {
            const int lam = 8 * kk + 2 * g + (i >> 1), e = (2 * i) & 3;
            const int row = 128 * (e >> 1) + 8 * (lam & 15) + (lam >> 4);
            const v2u r0 = *(const v2u*)(lds + L_WT + row * 32 + 2 * n16), r1 = *(const v2u*)(lds + L_WT + (row + 4) * 32 + 2 * n16);
            B[0][i] = plo(r0[0], r1[0]); B[1][i] = phi(r0[0], r1[0]);
            B[2][i] = plo(r0[1], r1[1]); B[3][i] = phi(r0[1], r1[1]);
        }
        asm volatile("" : "+v"(B[0]), "+v"(B[1]), "+v"(B[2]), "+v"(B[3]));
        if (wave == 15) {
            for (unsigned spins = 0;; ++spins) {
                const bool ok = st0[1] == tag;
                if (__all(ok) || spins > SPIN_CAP) break;
                CBAR();
                const v2u t2 = ld_g2(rs, ok ? 0xFFFFFF00u : af);
                st0[0] = ok ? st0[0] : t2[0]; st0[1] = ok ? st0[1] : t2[1];
            }
            const float ls = __uint_as_float(st0[0]);
            const float fe = __builtin_amdgcn_exp2f(ls - max_row16(ls));
            const float fw = fe * __builtin_amdgcn_rcpf(sum_row16(fe));
            if (lane < 32) ldf[L_FS + lane] = fw;
            asm volatile("s_waitcnt lgkmcnt(0)" ::: "memory");
            if (lane == 0) __hip_atomic_store(lds + L_FL + 4, 1u, __ATOMIC_RELAXED, __HIP_MEMORY_SCOPE_WORKGROUP);
        }
        for (unsigned spins = 0;; ++spins) {
            const bool ok = tagok(d0, tag) && tagok(d1, tag);
            if (__all(ok) || spins > SPIN_CAP) break;
            POLL_PAUSE();
            CBAR();
            repoll(rs, a0, d0, ok); repoll(rs, a0 + 16, d1, ok);
        }
        STAMP(6);
        const v4u a = {d0[0], d0[2], d1[0], d1[2]};
        const v4f z = {0.f, 0.f, 0.f, 0.f};
        const v4f D0 = mfma(a, B[0], z), D1 = mfma(a, B[1], z), D2 = mfma(a, B[2], z), D3 = mfma(a, B[3], z);
        SCHED();
        for (unsigned spins = 0; lds_ld_vol(lds + L_FL + 4) == 0u && spins < (SPIN_CAP << 8); ++spins) {}
        CBAR();
        const v4f fv = *(const v4f*)(ldf + L_FS + bb * 16 + 4 * g);
        const float f0 = fv[0], f1 = fv[1], f2 = fv[2], f3 = fv[3];
        const float y0 = fmaf(f3, D0[3], fmaf(f2, D0[2], fmaf(f1, D0[1], f0 * D0[0])));
        const float y1 = fmaf(f3, D1[3], fmaf(f2, D1[2], fmaf(f1, D1[1], f0 * D1[0])));
        const float y2 = fmaf(f3, D2[3], fmaf(f2, D2[2], fmaf(f1, D2[1], f0 * D2[0])));
        const float y3 = fmaf(f3, D3[3], fmaf(f2, D3[2], fmaf(f1, D3[1], f0 * D3[0])));
        const float y = tr16(tr32(y0, y2), tr32(y1, y3));
        ldf[L_RP + wave * 64 + 4 * n16 + g] = y;
        BARRIER();
        if (wave == 0) {
            const int b2 = g & 1;
            v4f acc = {0.f, 0.f, 0.f, 0.f};
#pragma unroll
            for (int k = 0; k < 8; ++k) acc += *(const v4f*)(ldf + L_RP + (2 * k + b2) * 64 + 4 * n16);
            const v4u gv = {pkbf(acc[0], acc[1]), tag, pkbf(acc[2], acc[3]), tag};
            if (lane < 32) st_g(rs, YG_OFF + ((((h * 7 + cc) * 2 + (n16 >> 3)) * 2 + b2) * 8 + (n16 & 7)) * 16, gv);
        }
        STAMP(7);
    } else if constexpr (ROLE == 2) {
        const int f = (j - 14) * 8 + kvh;
        v4u B1[4], B2[4];
        {
            unsigned* w = wt + (lane >> 3) * 32 + (lane & 7) * 4;
            *(v4u*)w = W0; *(v4u*)(w + 272) = W1; *(v4u*)(w + 544) = W2; *(v4u*)(w + 816) = W3;
            CBAR();
            const unsigned* r = wt + g * 272 + 2 * n16;
#pragma unroll
            for (int i = 0; i < 4; ++i) {
                const v2u r0 = *(const v2u*)(r + 64 * i), r1 = *(const v2u*)(r + 64 * i + 32);
                B1[0][i] = plo(r0[0], r1[0]); B1[1][i] = phi(r0[0], r1[0]); B1[2][i] = plo(r0[1], r1[1]); B1[3][i] = phi(r0[1], r1[1]);
            }
            asm volatile("" : "+v"(B1[0]), "+v"(B1[1]), "+v"(B1[2]), "+v"(B1[3]));
        }
        BARRIER();
        if (wave < 14) {
            unsigned* w = lds + L_W2 + wave * 1024 + lane * 4;
            *(v4u*)w = W4; *(v4u*)(w + 256) = W5; *(v4u*)(w + 512) = W6; *(v4u*)(w + 768) = W7;
        }
        BARRIER();
        {
            const unsigned* r = lds + L_W2 + g * 8 * 224 + (wave < 14 ? wave : 13) * 16 + n16;
#pragma unroll
            for (int i = 0; i < 4; ++i) {
                const unsigned a0 = r[(2 * i) * 224], a1 = r[(2 * i + 1) * 224], c0 = r[(32 + 2 * i) * 224], c1 = r[(33 + 2 * i) * 224];
                B2[0][i] = plo(a0, a1); B2[1][i] = phi(a0, a1); B2[2][i] = plo(c0, c1); B2[3][i] = phi(c0, c1);
            }
            asm volatile("" : "+v"(B2[0]), "+v"(B2[1]), "+v"(B2[2]), "+v"(B2[3]), "+v"(xres));
        }
        const v4f z4 = {0.f, 0.f, 0.f, 0.f};
        unsigned klo = 0x00003f80u, khi = 0x3f800000u;
        asm volatile("" : "+v"(klo), "+v"(khi));
        if (THR_B > 0 && wave == 15) {
            const unsigned af = YG_OFF + ((lane + f) % 112) * 512;
            for (unsigned spins = 0;; ++spins) {
                CBAR();
                const v2u fl = ld_g2(rs, af);
                if (__builtin_popcountll(__ballot(fl[1] == tag)) >= THR_B || spins > SPIN_CAP) break;
                __builtin_amdgcn_s_sleep(2);
            }
        }
        STAMP(10);
        if (THR_B > 0) BARRIER();
        STAMP(11);
        if (wave < 14) {
            const unsigned ay = YG_OFF + (((g * 7 + (wave >> 1)) * 2 + (wave & 1)) * 16 + n16) * 16;
            constexpr unsigned HS = 4 * 7 * 512;
            v4u y[4];
#pragma unroll
            for (int i = 0; i < 4; ++i) y[i] = ld_g(rs, ay + i * HS);
            for (unsigned spins = 0;; ++spins) {
                const bool k0 = tagok(y[0], tag), k1 = tagok(y[1], tag), k2 = tagok(y[2], tag), k3 = tagok(y[3], tag);
                if (__all(k0 && k1 && k2 && k3) || spins > SPIN_CAP) break;
                POLL_PAUSE();
                CBAR();
                repoll(rs, ay, y[0], k0); repoll(rs, ay + HS, y[1], k1); repoll(rs, ay + 2 * HS, y[2], k2); repoll(rs, ay + 3 * HS, y[3], k3);
            }
            STAMP(6);
            float c0 = 0.f, c1 = 0.f, c2 = 0.f, c3 = 0.f;
#pragma unroll
            for (int i = 0; i < 4; ++i) {
                c0 = dot2(y[i][0], klo, c0); c1 = dot2(y[i][0], khi, c1);
                c2 = dot2(y[i][2], klo, c2); c3 = dot2(y[i][2], khi, c3);
            }
            const float ys = tr16(tr32(c0, c2), tr32(c1, c3));
            const unsigned hb16 = pkbf(bflo(xres) + rbf(ys), 0.f);
            ((u16*)(lds + L_HB))[(bit3 ? DM : 0) + wave * 32 + (lane & 7) * 4 + g] = (u16)hb16;
            CBAR();
            const v4u av = *(const v4u*)(lds + (n16 == 2 ? L_ONES : L_HB + (n16 & 1) * (DM / 2) + wave * 16 + g * 4));
            const v4f D0 = mfma(av, B1[0], z4), D1 = mfma(av, B1[1], z4), D2 = mfma(av, B1[2], z4), D3 = mfma(av, B1[3], z4);
            const v4f DS = mfma(av, av, z4);
            if (lane < 2) *(v2f*)(ldf + L_ST + (lane * 16 + wave) * 2) = (v2f){DS[2], lane ? DS[1] : DS[0]};
            if (lane < 16) {
                float* zp = ldf + L_RP + (wave * 64 + n16) * 4;
                *(v4f*)zp = D0; *(v4f*)(zp + 64) = D1; *(v4f*)(zp + 128) = D2; *(v4f*)(zp + 192) = D3;
            }
        }
        BARRIER();
        if (wave != 0) STAMP(12);
        if (wave < 4) {
            const float* zp = ldf + L_RP + (wave * 16 + n16) * 4;
            const v2f sv = *(const v2f*)(n16 < 14 ? ldf + L_ST + ((g >> 1) * 16 + n16) * 2 : ldf + L_ZERO);
            v4f zs = *(const v4f*)(zp + g * 256) + *(const v4f*)(zp + (g + 4) * 256) + *(const v4f*)(zp + (g + 8) * 256);
            const v4f zl = *(const v4f*)(g < 2 ? zp + (g + 12) * 256 : ldf + L_ZERO);
            zs += zl;
            const float mean = sum_row16(sv[0]) * (1.f / DM);
            const float var = fmaf(-mean, mean, sum_row16(sv[1]) * (1.f / DM));
            float mb, idn;
            ln_params(mean, fmaxf(var, 0.f), mb, idn);
            const float zb = sum16x(tr32(zs[0], zs[1])), cs = sum16x(sum32x(zs[2]));
            const float zz = rbf((zb - mb * cs) * idn);
            const float act = zz * __builtin_amdgcn_rcpf(1.f + __builtin_amdgcn_exp2f(zz * -1.4426950408889634f));
            if (!bit4) ((u16*)(lds + L_AA))[(g >> 1) * 64 + 4 * n16 + wave] = (u16)pkbf(act, act);
        }
        BARRIER();
        STAMP(13);
        if (wave < 14) {
            __builtin_amdgcn_s_setprio(3);
            const v4u a0 = *(const v4u*)(lds + L_AA + (n16 & 1) * 32 + g * 4), a1 = *(const v4u*)(lds + L_AA + (n16 & 1) * 32 + 16 + g * 4);
            const v4f D0 = mfma(a1, B2[2], mfma(a0, B2[0], z4)), D1 = mfma(a1, B2[3], mfma(a0, B2[1], z4));
            const v4u g0 = {pkbf(D0[0], D1[0]), tag, pkbf(D0[1], D1[1]), tag};
            if (lane < 16) st_g(rs, OG_OFF + (f * 224 + wave * 16 + n16) * 16, g0);
            __builtin_amdgcn_s_setprio(0);
        }
        STAMP(7);
        if (wave >= 16 - NFIN) {
            for (int i = 0; i < 15 - wave; ++i) __builtin_amdgcn_s_sleep(FSTAG);
            const unsigned a = OG_OFF + ((lane >> 3) * 224 + f * 8 + (lane & 7)) * 16;
            constexpr unsigned FS = 8 * 224 * 16;
            const int col = (lane >> 5) * DM + f * 16 + 2 * (lane & 7);
            const unsigned hh2 = lds[L_HB + col / 2];
            v4u gv[4];
#pragma unroll
            for (int i = 0; i < 3; ++i) gv[i] = ld_g(rs, a + i * FS);
            const unsigned a3 = lane < 32 ? a + 3 * FS : a;
            gv[3] = ld_g(rs, a3);
            for (unsigned spins = 0;; ++spins) {
                const bool k0 = tagok(gv[0], tag), k1 = tagok(gv[1], tag), k2 = tagok(gv[2], tag), k3 = tagok(gv[3], tag);
                if (__all(k0 && k1 && k2 && k3) || spins > SPIN_CAP) break;
                CBAR();
                repoll(rs, a, gv[0], k0); repoll(rs, a + FS, gv[1], k1); repoll(rs, a + 2 * FS, gv[2], k2); repoll(rs, a3, gv[3], k3);
            }
            STAMP(8);
            if (lane >= 32) { gv[3][0] = 0u; gv[3][2] = 0u; }
            float c0 = 0.f, c1 = 0.f, c2 = 0.f, c3 = 0.f;
#pragma unroll
            for (int i = 0; i < 4; ++i) {
                c0 = dot2(gv[i][0], klo, c0); c1 = dot2(gv[i][0], khi, c1);
                c2 = dot2(gv[i][2], klo, c2); c3 = dot2(gv[i][2], khi, c3);
            }
            float s0 = sum16x(tr32(c0, c2)), s1 = sum16x(tr32(c1, c3));
            s0 += dpp<ROR8>(s0); s1 += dpp<ROR8>(s1);
            if ((lane & 24) == 0) {
                *(GAS unsigned*)(out + col) = pkbf(bflo(hh2) + rbf(s0), bfhi(hh2) + rbf(s1));
                if (f == 0 && lane == 0 && wave == 15) __hip_atomic_store((GAS unsigned*)(ws + E_OFF), tag, __ATOMIC_RELAXED, __HIP_MEMORY_SCOPE_AGENT);
            }
        }
        STAMP(9);
    }
}

__global__ void __launch_bounds__(NTHR, 4)
fused_decode(const u16* __restrict__ x, const u16* __restrict__ kc, const u16* __restrict__ vc, const u16* __restrict__ wq,
             const u16* __restrict__ wo, const u16* __restrict__ w1, const u16* __restrict__ w2, u16* __restrict__ out, char* ws) {
    __shared__ __attribute__((aligned(16))) unsigned lds[L_END];
    const int wave = __builtin_amdgcn_readfirstlane(threadIdx.x >> 6), lane = threadIdx.x & 63;
    const int kvh = blockIdx.x & 7, j = blockIdx.x >> 3;
    const int role = j < 14 ? 1 : ((j - 14) * 8 + kvh < NF ? 2 : 0);
    if (role == 1) body<1>(x, kc, vc, wq, wo, w1, w2, out, ws, lds, kvh, j, wave, lane);
    else if (role == 2) body<2>(x, kc, vc, wq, wo, w1, w2, out, ws, lds, kvh, j, wave, lane);
    else body<0>(x, kc, vc, wq, wo, w1, w2, out, ws, lds, kvh, j, wave, lane);
}

extern "C" void kernel_launch(void* const* d_in, const int* in_sizes, int n_in, void* d_out, int out_size, void* d_ws,
                              size_t ws_size, hipStream_t stream) {
    fused_decode<<<256, NTHR, 0, stream>>>((const u16*)d_in[0], (const u16*)d_in[1], (const u16*)d_in[2], (const u16*)d_in[3],
                                           (const u16*)d_in[4], (const u16*)d_in[6], (const u16*)d_in[7], (u16*)d_out, (char*)d_ws);
}
```

```cpp
#include <hip/hip_runtime.h>
#include <hip/hip_bf16.h>
#include <stdint.h>

typedef unsigned short u16;
typedef unsigned v4u __attribute__((ext_vector_type(4)));
typedef unsigned v2u __attribute__((ext_vector_type(2)));
typedef float v4f __attribute__((ext_vector_type(4)));
typedef float v2f __attribute__((ext_vector_type(2)));
typedef __bf16 bf16x2 __attribute__((ext_vector_type(2)));
typedef __bf16 bf16x8 __attribute__((ext_vector_type(8)));
#define GAS __attribute__((address_space(1)))

constexpr int DM = 448, NQ = 4096, SK = 4096, DH = 256, FF = 1792;
constexpr int NTHR = 1024;

constexpr unsigned E_OFF = 0;
constexpr unsigned QG_OFF = 4096;
constexpr unsigned AG_OFF = QG_OFF + 131072;
constexpr unsigned ST_OFF = AG_OFF + 524288;
constexpr unsigned YG_OFF = ST_OFF + 65536;
constexpr unsigned OG_OFF = YG_OFF + 57344;
constexpr unsigned WS_USED = OG_OFF + 100352;
constexpr int NF = 28;
constexpr unsigned SPIN_CAP = 1u << 17;

constexpr int L_QS = 0;
constexpr int L_ZERO = 260;
constexpr int L_ONES = 264;
constexpr int L_MF = 268;
constexpr int L_ML = 272;
constexpr int L_PA = L_ML + 64;
constexpr int L_QP = L_PA + 256;
constexpr int L_HB = L_QP + 448;
constexpr int L_ST = L_HB + 448;
constexpr int L_AA = L_ST + 128;
constexpr int L_XN = L_AA + 64;
constexpr int L_RP = L_XN + 448;
constexpr int L_FL = L_RP + 3584;
constexpr int L_FS = L_FL + 16;
constexpr int TILE_DW = 576;
constexpr int L_WT = L_FS + 32;
constexpr int L_MB = L_WT + 16 * 2 * TILE_DW;
constexpr int L_HK = L_MB + 8192;
constexpr int L_W2 = L_MB;
constexpr int L_END = L_MB + 14336;

__device__ __forceinline__ float bflo(unsigned v) { return __uint_as_float(v << 16); }
__device__ __forceinline__ float bfhi(unsigned v) { return __uint_as_float(v & 0xffff0000u); }
__device__ __forceinline__ unsigned pkbf(float lo, float hi) {
    bf16x2 r; r[0] = (__bf16)lo; r[1] = (__bf16)hi; return __builtin_bit_cast(unsigned, r);
}
__device__ __forceinline__ float rbf(float f) { return bflo(pkbf(f, f)); }
__device__ __forceinline__ float dot2(unsigned a, unsigned b, float acc) {
    return __builtin_amdgcn_fdot2_f32_bf16(__builtin_bit_cast(bf16x2, a), __builtin_bit_cast(bf16x2, b), acc, false);
}
__device__ __forceinline__ v4f mfma(v4u a, v4u b, v4f c) {
    return __builtin_amdgcn_mfma_f32_16x16x32_bf16(__builtin_bit_cast(bf16x8, a), __builtin_bit_cast(bf16x8, b), c, 0, 0, 0);
}
__device__ __forceinline__ unsigned plo(unsigned r0, unsigned r1) { return __builtin_amdgcn_perm(r1, r0, 0x05040100u); }
__device__ __forceinline__ unsigned phi(unsigned r0, unsigned r1) { return __builtin_amdgcn_perm(r1, r0, 0x07060302u); }
template <int CTRL> __device__ __forceinline__ float dpp(float x) {
    return __builtin_bit_cast(float, __builtin_amdgcn_mov_dpp(__builtin_bit_cast(int, x), CTRL, 0xf, 0xf, true));
}
constexpr int XOR1 = 0xB1, XOR2 = 0x4E, XOR7 = 0x141, ROR8 = 0x128, ROR4 = 0x124;
__device__ __forceinline__ float tr32(float a, float b) {
    auto s = __builtin_amdgcn_permlane32_swap(__float_as_uint(a), __float_as_uint(b), false, false);
    return __uint_as_float(s[0]) + __uint_as_float(s[1]);
}
__device__ __forceinline__ float tr16(float a, float b) {
    auto s = __builtin_amdgcn_permlane16_swap(__float_as_uint(a), __float_as_uint(b), false, false);
    return __uint_as_float(s[0]) + __uint_as_float(s[1]);
}
template <int CTRL> __device__ __forceinline__ float trdpp(float a, float b, bool bit) {
    const float keep = bit ? b : a, send = bit ? a : b;
    return keep + dpp<CTRL>(send);
}
__device__ __forceinline__ float sum32x(float x) { return tr32(x, x); }
__device__ __forceinline__ float sum16x(float x) { return tr16(x, x); }
__device__ __forceinline__ float sum_row16(float x) {
    x += dpp<XOR1>(x); x += dpp<XOR2>(x); x += dpp<XOR7>(x); x += dpp<ROR8>(x); return x;
}
__device__ __forceinline__ float max_row16(float x) {
    x = fmaxf(x, dpp<XOR1>(x)); x = fmaxf(x, dpp<XOR2>(x)); x = fmaxf(x, dpp<XOR7>(x)); x = fmaxf(x, dpp<ROR8>(x)); return x;
}
__device__ __forceinline__ float sum_wave(float x) { return sum32x(sum16x(sum_row16(x))); }

#define BARRIER() asm volatile("s_waitcnt lgkmcnt(0)\n\ts_barrier" ::: "memory")
#define CBAR() asm volatile("" ::: "memory")
#define SCHED() __builtin_amdgcn_sched_barrier(0)

__device__ __forceinline__ v4u ld_nt(const void* p) { return __builtin_nontemporal_load((const GAS v4u*)p); }
__device__ __forceinline__ v4u ld_g(__amdgpu_buffer_rsrc_t r, unsigned off) { return __builtin_amdgcn_raw_buffer_load_b128(r, off, 0, 16); }
__device__ __forceinline__ v2u ld_g2(__amdgpu_buffer_rsrc_t r, unsigned off) { return __builtin_amdgcn_raw_buffer_load_b64(r, off, 0, 16); }
__device__ __forceinline__ void st_g(__amdgpu_buffer_rsrc_t r, unsigned off, v4u v) { __builtin_amdgcn_raw_buffer_store_b128(v, r, off, 0, 16); }
__device__ __forceinline__ void st_g2(__amdgpu_buffer_rsrc_t r, unsigned off, v2u v) { __builtin_amdgcn_raw_buffer_store_b64(v, r, off, 0, 16); }
__device__ __forceinline__ bool tagok(v4u v, unsigned tag) { return v[1] == tag && v[3] == tag; }
__device__ __forceinline__ unsigned lds_ld_vol(unsigned* p) { return __hip_atomic_load(p, __ATOMIC_RELAXED, __HIP_MEMORY_SCOPE_WORKGROUP); }

__device__ __forceinline__ void ln_params(float mean, float var, float& mb, float& inv_den) {
    mb = rbf(mean);
    inv_den = __builtin_amdgcn_rcpf(rbf(__builtin_amdgcn_sqrtf(rbf(rbf(var) + 1e-5f))));
}
__device__ __forceinline__ void tile_frags(unsigned* t, int lane, v4u r0, v4u r1, v4u& f0, v4u& f1) {
    const int row = lane >> 2;
    unsigned* w = t + row * 16 + (row >> 3) * 16 + (lane & 3) * 4;
    *(v4u*)w = r0; *(v4u*)(w + 288) = r1;
    CBAR();
    const unsigned* r = t + (lane >> 4) * 144 + (lane & 15);
#pragma unroll
    for (int i = 0; i < 4; ++i) {
        const unsigned a = r[32 * i], c = r[32 * i + 16];
        f0[i] = plo(a, c); f1[i] = phi(a, c);
    }
}
__device__ __forceinline__ void repoll(__amdgpu_buffer_rsrc_t r, unsigned off, v4u& v, bool ok) {
    const v4u t = ld_g(r, ok ? 0xFFFFFF00u : off);
#pragma unroll
    for (int i = 0; i < 4; ++i) v[i] = ok ? v[i] : t[i];
}
constexpr int QX = 3;

__host__ __device__ __forceinline__ void role_of(int xl, int j, int& role, int& rp0, int& rp1) {
    role = 0; rp0 = 0; rp1 = 0;
    if (xl >= 6) { if (j < 14) { role = 2; rp0 = (xl - 6) * 14 + j; } }
    else if (j < 21) {
        const int gsv = (xl + 1 + j % 7) & 7, k = j / 7;
        const int tid = gsv < 6 ? k * 5 + (xl < gsv ? xl : xl - 1) : k * 6 + xl;
        if (tid < 14) { role = 1; rp0 = gsv * 2 + tid / 7; rp1 = tid % 7; }
    } else { const int u = (j - 21) * 6 + xl; if (u < NF) { role = 3; rp0 = u; } }
}

template <int ROLE>
__device__ __forceinline__ void body(const u16* __restrict__ x, const u16* __restrict__ kc, const u16* __restrict__ vc,
                                     const u16* __restrict__ wq, const u16* __restrict__ wo, const u16* __restrict__ w1,
                                     const u16* __restrict__ w2, u16* __restrict__ out, char* ws, unsigned* lds,
                                     const int kvh, const int j, const int rp0, const int rp1, const int wave, const int lane) {
    const __amdgpu_buffer_rsrc_t rs = __builtin_amdgcn_make_buffer_rsrc(ws, 0, WS_USED, 0x00020000);
    float* ldf = (float*)lds;
    __builtin_amdgcn_s_setprio(3);
    const int b = j >> 4, s = j & 15;
    const int n16 = lane & 15, g = lane >> 4;
    const bool bit3 = (lane & 8) != 0, bit4 = (lane & 16) != 0;
    unsigned* wt = lds + L_WT + wave * (2 * TILE_DW);
    unsigned epoch = 0;
    v4u K[8], V[8];
    v4u W0 = {0, 0, 0, 0}, W1 = W0, W2 = W0, W3 = W0; unsigned xres = 0;
    unsigned stmp;
    {
        const size_t kvrow = (size_t)(b * 8 + kvh) * SK + s * 256;
        const unsigned ko = ((n16 & 7) * DH + ((n16 >> 3) * 4 + g) * 8) * 2, ko2 = ko + 4096;
        const unsigned vo = lane * 16, vo2 = vo + 4096;
        const u16* kb = kc + (kvrow + wave * 16) * DH;
        const u16* vb = vc + (kvrow + wave * 16) * DH;
#define KLD0 "global_load_dwordx4 %0, %[ko], %[kb] nt\n\t"
#define KLD1 "global_load_dwordx4 %1, %[ko], %[kb] offset:128 nt\n\t"
#define KLD2 "global_load_dwordx4 %2, %[ko], %[kb] offset:256 nt\n\t"
#define KLD3 "global_load_dwordx4 %3, %[ko], %[kb] offset:384 nt\n\t"
#define KV_LOADS_A KLD0 KLD1
#define KV_LOADS_A2 KLD2 KLD3
#define KV_LOADS_B \
            "global_load_dwordx4 %4, %[ko2], %[kb] nt\n\tglobal_load_dwordx4 %5, %[ko2], %[kb] offset:128 nt\n\t" \
            "global_load_dwordx4 %6, %[ko2], %[kb] offset:256 nt\n\tglobal_load_dwordx4 %7, %[ko2], %[kb] offset:384 nt\n\t" \
            "global_load_dwordx4 %8, %[vo], %[vb] nt\n\tglobal_load_dwordx4 %9, %[vo], %[vb] offset:1024 nt\n\t" \
            "global_load_dwordx4 %10, %[vo], %[vb] offset:2048 nt\n\tglobal_load_dwordx4 %11, %[vo], %[vb] offset:3072 nt\n\t" \
            "global_load_dwordx4 %12, %[vo2], %[vb] nt\n\tglobal_load_dwordx4 %13, %[vo2], %[vb] offset:1024 nt\n\t" \
            "global_load_dwordx4 %14, %[vo2], %[vb] offset:2048 nt\n\tglobal_load_dwordx4 %15, %[vo2], %[vb] offset:3072 nt\n\t"
#define KV_LOADS KV_LOADS_A KV_LOADS_A2 KV_LOADS_B
#define KV_OUT(c) c(K[0]), c(K[1]), c(K[2]), c(K[3]), c(K[4]), c(K[5]), c(K[6]), c(K[7]), c(V[0]), c(V[1]), c(V[2]), c(V[3]), c(V[4]), c(V[5]), c(V[6]), c(V[7])
        {
            const int i = (wave - 8) & 7;
            const u16* hk = kc + kvrow * DH + (i >> 2) * 8 * DH + (i & 3) * 64;
            const u16* hv = vc + kvrow * DH + i * 512;
            const unsigned lk = (unsigned)(size_t)(__attribute__((address_space(3))) unsigned*)(lds + L_HK + i * 256);
            unsigned keep;
            asm volatile("s_cmp_lt_u32 %[wv], 8\n\ts_cbranch_scc1 1f\n\t"
                         "s_setprio 1\n\t"
                         "s_mov_b32 %[keep], m0\n\ts_mov_b32 m0, %[lk]\n\ts_nop 0\n\tglobal_load_lds_dwordx4 %[ko], %[hk] nt\n\t"
                         "s_add_u32 m0, m0, 0x2000\n\ts_nop 0\n\tglobal_load_lds_dwordx4 %[vo], %[hv] nt\n\ts_mov_b32 m0, %[keep]\n\t"
                         KV_LOADS_A
                         "1:"
                         : KV_OUT("=&v"), [keep] "=&s"(keep)
                         : [wv] "s"(wave), [ko] "v"(ko), [ko2] "v"(ko2), [vo] "v"(vo), [vo2] "v"(vo2), [kb] "s"(kb), [vb] "s"(vb),
                           [hk] "s"(hk), [hv] "s"(hv), [lk] "s"(lk)
                         : "memory", "scc");
        }
        v4u xf = {0, 0, 0, 0}, xa = {0, 0, 0, 0};
        if (wave < 8) {
            const int lc = lane < 56 ? lane : 55;
            const u16* p0 = wave ? wq + (size_t)((j >> 4) * 224 + (wave - 1) * 32 + (lane >> 2)) * NQ + ((kvh + QX) & 7) * 512 + (j & 15) * 32 + (lane & 3) * 8 : x + lc * 8;
            const u16* p1 = wave ? p0 + (size_t)16 * NQ : p0 + DM;
            asm volatile("s_cmp_eq_u32 %[wv], 0\n\ts_cbranch_scc1 1f\n\t"
                         "global_load_dwordx4 %0, %3, off nt\n\tglobal_load_dwordx4 %1, %4, off nt\n\ts_branch 2f\n"
                         "1:\n\tglobal_load_dwordx4 %0, %3, off\n\tglobal_load_dwordx4 %1, %4, off\n"
                         "2:\n\tglobal_load_dword %2, %[zero], %[wsb] sc1"
                         : "=&v"(xf), "=&v"(xa), "+v"(epoch) : "v"(p0), "v"(p1), [wv] "s"(wave), [zero] "v"(0u), [wsb] "s"(ws) : "memory", "scc");
            if (wave == 0) {
                if (lane < 12) lds[L_ZERO + lane] = (lane >= 4 && lane < 8) ? 0x3f803f80u : ((lane == 8 || lane == 9) ? 0xff800000u : 0u);
                if (lane < 16) lds[L_FL + lane] = 0u;
            }
        }
        SCHED();
        asm volatile("s_waitcnt lgkmcnt(0)\n\ts_barrier" ::: "memory");
        asm volatile("s_cmp_lt_u32 %[wv], 8\n\ts_cbranch_scc1 1f\n\t"
                     KV_LOADS_A2
                     "global_load_dword %[ep], %[zero], %[wsb] sc1\n\t"
                     KV_LOADS_B
                     "1:"
                     : KV_OUT("+v"), [ep] "+v"(epoch)
                     : [wv] "s"(wave), [ko] "v"(ko), [ko2] "v"(ko2), [vo] "v"(vo), [vo2] "v"(vo2), [kb] "s"(kb), [vb] "s"(vb), [zero] "v"(0u), [wsb] "s"(ws)
                     : "memory", "scc");
        if (wave < 8) {
            asm volatile("s_waitcnt vmcnt(0)" : "+v"(xf), "+v"(xa), "+v"(epoch));
            const unsigned tag = epoch + 1u;
            if (wave) {
                v4u b0, b1;
                tile_frags(wt, lane, xf, xa, b0, b1);
                asm volatile("" : "+v"(b0), "+v"(b1));
                for (unsigned spins = 0; lds_ld_vol(lds + L_FL) == 0u && spins < (SPIN_CAP << 6); ++spins) __builtin_amdgcn_s_sleep(1);
                CBAR();
                const v4u a0 = *(const v4u*)(lds + L_XN + (n16 & 1) * (DM / 2) + (j >> 4) * 112 + (wave - 1) * 16 + g * 4);
                const v4f z = {0.f, 0.f, 0.f, 0.f};
                const v4f d0 = mfma(a0, b0, z), d1 = mfma(a0, b1, z);
                if (lane < 16) *(v4f*)(ldf + L_QP + ((wave - 1) * 16 + n16) * 4) = (v4f){d0[0], d1[0], d0[1], d1[1]};
                asm volatile("s_waitcnt lgkmcnt(0)" ::: "memory");
                if (lane == 0) __hip_atomic_fetch_add(lds + L_FL + 1, 1u, __ATOMIC_RELAXED, __HIP_MEMORY_SCOPE_WORKGROUP);
            } else {
                const float msk = lane < 56 ? 1.f : 0.f;
                float s0 = 0.f, s1 = 0.f;
#pragma unroll
                for (int k = 0; k < 4; ++k) { s0 += bflo(xf[k]) + bfhi(xf[k]); s1 += bflo(xa[k]) + bfhi(xa[k]); }
                s0 = sum_wave(s0 * msk) * (1.f / DM); s1 = sum_wave(s1 * msk) * (1.f / DM);
                float v0 = 0.f, v1 = 0.f;
#pragma unroll
                for (int k = 0; k < 4; ++k) {
                    float d;
                    d = bflo(xf[k]) - s0; v0 = fmaf(d, d, v0); d = bfhi(xf[k]) - s0; v0 = fmaf(d, d, v0);
                    d = bflo(xa[k]) - s1; v1 = fmaf(d, d, v1); d = bfhi(xa[k]) - s1; v1 = fmaf(d, d, v1);
                }
                v0 = sum_wave(v0 * msk) * (1.f / DM); v1 = sum_wave(v1 * msk) * (1.f / DM);
                float mb0, id0, mb1, id1;
                ln_params(s0, v0, mb0, id0); ln_params(s1, v1, mb1, id1);
                v4u n0, n1;
#pragma unroll
                for (int k = 0; k < 4; ++k) {
                    const unsigned c0 = pkbf(bflo(xf[k]) - mb0, bfhi(xf[k]) - mb0), c1 = pkbf(bflo(xa[k]) - mb1, bfhi(xa[k]) - mb1);
                    n0[k] = pkbf(bflo(c0) * id0, bfhi(c0) * id0);
                    n1[k] = pkbf(bflo(c1) * id1, bfhi(c1) * id1);
                }
                if (lane < 56) { *(v4u*)(lds + L_XN + lane * 4) = n0; *(v4u*)(lds + L_XN + DM / 2 + lane * 4) = n1; }
                asm volatile("s_waitcnt lgkmcnt(0)" ::: "memory");
                if (lane == 0) __hip_atomic_store(lds + L_FL, 1u, __ATOMIC_RELAXED, __HIP_MEMORY_SCOPE_WORKGROUP);
                for (unsigned spins = 0; lds_ld_vol(lds + L_FL + 1) < 7u && spins < (SPIN_CAP << 6); ++spins) {}
                CBAR();
                {
                    v2f t0 = {0.f, 0.f};
#pragma unroll
                    for (int w = 0; w < 7; ++w) t0 += *(const v2f*)(ldf + L_QP + (w * 16 + n16) * 4 + (g & 1) * 2);
                    const v4u gq = {__float_as_uint(t0[0]), tag, __float_as_uint(t0[1]), tag};
                    if (lane < 32) st_g(rs, QG_OFF + ((((kvh + QX) & 7) * 32 + j) * 32 + lane) * 16, gq);
                }
                const unsigned base = QG_OFF + ((kvh * 32 + g) * 32 + b * 16 + n16) * 16;
                v4u gg[8];
                __builtin_amdgcn_s_sleep(8);
                for (unsigned spins = 0;; ++spins) {
                    CBAR();
                    bool ok = true;
#pragma unroll
                    for (int i = 0; i < 8; ++i) gg[i] = ld_g(rs, base + i * 2048);
#pragma unroll
                    for (int i = 0; i < 8; ++i) ok &= tagok(gg[i], tag);
                    if (__all(ok) || spins > SPIN_CAP) break;
                }
#pragma unroll
                for (int m = 0; m < 4; ++m)
                    lds[L_QS + m * 64 + lane] = pkbf(__uint_as_float(gg[m][0]) + __uint_as_float(gg[4 + m][0]), __uint_as_float(gg[m][2]) + __uint_as_float(gg[4 + m][2]));
                asm volatile("s_waitcnt lgkmcnt(0)" ::: "memory");
                if (lane == 0) __hip_atomic_store(lds + L_FL + 2, 1u, __ATOMIC_RELAXED, __HIP_MEMORY_SCOPE_WORKGROUP);
                for (unsigned spins = 0; lds_ld_vol(lds + L_FL + 3) < 8u && spins < (SPIN_CAP << 6); ++spins) __builtin_amdgcn_s_sleep(2);
            }
            __builtin_amdgcn_s_waitcnt(0x0F70);
        }
        asm volatile("s_sub_u32 %[t], %[wv], 1\n\ts_cmp_gt_u32 %[t], 6\n\ts_cbranch_scc1 1f\n\t"
                     "s_setprio 1\n\t"
                     KV_LOADS
                     "1:\n\ts_setprio 0\n\t"
                     "s_cmp_lg_u32 %[wv], 0\n\ts_cbranch_scc1 2f\n\t"
                     "ds_read_b128 %0, %[hl]\n\tds_read_b128 %1, %[hl] offset:1024\n\tds_read_b128 %2, %[hl] offset:2048\n\tds_read_b128 %3, %[hl] offset:3072\n\t"
                     "ds_read_b128 %4, %[hl] offset:4096\n\tds_read_b128 %5, %[hl] offset:5120\n\tds_read_b128 %6, %[hl] offset:6144\n\tds_read_b128 %7, %[hl] offset:7168\n\t"
                     "ds_read_b128 %8, %[hl] offset:8192\n\tds_read_b128 %9, %[hl] offset:9216\n\tds_read_b128 %10, %[hl] offset:10240\n\tds_read_b128 %11, %[hl] offset:11264\n\t"
                     "ds_read_b128 %12, %[hl] offset:12288\n\tds_read_b128 %13, %[hl] offset:13312\n\tds_read_b128 %14, %[hl] offset:14336\n\tds_read_b128 %15, %[hl] offset:15360\n\t"
                     "s_waitcnt lgkmcnt(0)\n\t"
                     "2:"
                     : KV_OUT("+v"), [t] "=&s"(stmp)
                     : [wv] "s"(wave), [ko] "v"(ko), [ko2] "v"(ko2), [vo] "v"(vo), [vo2] "v"(vo2), [kb] "s"(kb), [vb] "s"(vb),
                       [hl] "v"((unsigned)(size_t)(__attribute__((address_space(3))) unsigned*)(lds + L_HK) + lane * 16)
                     : "memory", "scc");
        if (wave >= 8) {
            asm volatile("s_waitcnt vmcnt(17)" ::: "memory");
            if (lane == 0) __hip_atomic_fetch_add(lds + L_FL + 3, 1u, __ATOMIC_RELAXED, __HIP_MEMORY_SCOPE_WORKGROUP);
        }
        for (unsigned spins = 0; lds_ld_vol(lds + L_FL + 2) == 0u && spins < (SPIN_CAP << 6); ++spins) __builtin_amdgcn_s_sleep(2);
        CBAR();
    }
    float t;
    {
        const unsigned* qa = lds + L_QS + (n16 & 1) * 128 + (((n16 >> 1) & 1) * 4 + g) * 4;
        v4f d0 = {0.f, 0.f, 0.f, 0.f}, d1 = {0.f, 0.f, 0.f, 0.f};
        v4u qf[4];
#pragma unroll
        for (int i = 0; i < 4; ++i) qf[i] = *(const v4u*)(qa + i * 32);
        asm volatile("s_waitcnt vmcnt(8)" : "+v"(K[0]), "+v"(K[1]), "+v"(K[2]), "+v"(K[3]), "+v"(K[4]), "+v"(K[5]), "+v"(K[6]), "+v"(K[7]));
#pragma unroll
        for (int i = 0; i < 4; ++i) { d0 = mfma(qf[i], K[i], d0); d1 = mfma(qf[i], K[4 + i], d1); }
        const float s0 = trdpp<ROR8>(bit3 ? d0[2] : d0[0], bit3 ? d1[2] : d1[0], bit3);
        const float s1 = trdpp<ROR8>(bit3 ? d0[3] : d0[1], bit3 ? d1[3] : d1[1], bit3);
        t = rbf(bit4 ? s1 : s0) * (0.0625f * 1.4426950408889634f);
    }
    {
        const float mw = max_row16(t);
        const float p = __builtin_amdgcn_exp2f(t - mw);
        if (lane < 32) ((u16*)(lds + L_PA + wave * 16))[(g * 2 + (n16 & 1)) * 8 + (n16 >> 1)] = (u16)pkbf(p, p);
        CBAR();
        const float lw = sum_row16(p);
        if ((lane & 47) == 0) {
            ldf[L_ML + (lane >> 4) * 16 + wave] = mw; ldf[L_ML + 32 + (lane >> 4) * 16 + wave] = lw;
            __hip_atomic_fetch_max(ldf + L_MF + (lane >> 4), mw, __ATOMIC_RELAXED, __HIP_MEMORY_SCOPE_WORKGROUP);
        }
    }
    v4u PA[4];
#pragma unroll
    for (int tb = 0; tb < 4; ++tb) {
        const bool on = (n16 >> 2) == tb && ((n16 >> 1) & 1) == (g & 1);
        PA[tb] = *(const v4u*)(lds + (on ? L_PA + wave * 16 + ((n16 & 1) * 2 + (g >> 1)) * 4 : L_ZERO));
    }
    {
        v4f acc0 = {0.f, 0.f, 0.f, 0.f}, acc1 = {0.f, 0.f, 0.f, 0.f};
        asm volatile("s_waitcnt vmcnt(0)" : "+v"(V[0]), "+v"(V[1]), "+v"(V[2]), "+v"(V[3]), "+v"(V[4]), "+v"(V[5]), "+v"(V[6]), "+v"(V[7]), "+v"(epoch));
#pragma unroll
        for (int tt = 0; tt < 8; ++tt) {
            v4u bt;
#pragma unroll
            for (int ii = 0; ii < 4; ++ii)
                bt[ii] = (tt & 1) ? phi(V[2 * ii][tt >> 1], V[2 * ii + 1][tt >> 1]) : plo(V[2 * ii][tt >> 1], V[2 * ii + 1][tt >> 1]);
            if (tt < 4) acc0 = mfma(PA[tt & 3], bt, acc0); else acc1 = mfma(PA[tt & 3], bt, acc1);
        }
        *(v4f*)(ldf + L_MB + ((wave * 2 + 0) * 64 + lane) * 4) = acc0;
        *(v4f*)(ldf + L_MB + ((wave * 2 + 1) * 64 + lane) * 4) = acc1;
        asm volatile("s_waitcnt lgkmcnt(0)" ::: "memory");
        if (lane == 0) __hip_atomic_fetch_add(lds + L_MF + 2, 1u, __ATOMIC_RELAXED, __HIP_MEMORY_SCOPE_WORKGROUP);
    }
    const unsigned tag = epoch + 1u;
    if constexpr (ROLE == 1) {
        if (wave >= 4 && wave < 12) {
            const int h = rp0, cc = rp1;
            const u16* p = wo + (size_t)(h * 256 + (wave - 4) * 32 + (lane >> 3)) * DM + cc * 64 + (lane & 7) * 8;
            W0 = ld_nt(p); W1 = ld_nt(p + 8 * DM); W2 = ld_nt(p + 16 * DM); W3 = ld_nt(p + 24 * DM);
        }
    }
    SCHED();
    if (wave < 4) {
        __builtin_amdgcn_s_setprio(3);
        const int lam = wave * 16 + n16;
        unsigned opub = !bit4 ? AG_OFF + ((((kvh * 2 + (g >> 1)) * 2 + b) * 16 + s) * 64 + lam) * 16 : ST_OFF + (((kvh * 2 + (g >> 1)) * 2 + b) * 16 + s) * 128;
        asm volatile("" : "+v"(opub));
        for (unsigned spins = 0; lds_ld_vol(lds + L_MF + 2) < 16u && spins < (SPIN_CAP << 8); ++spins) {}
        CBAR();
        const v2f MM = *(const v2f*)(ldf + L_MF);
        const float mo = ldf[L_ML + (g >> 1) * 16 + n16];
        const float lw = ldf[L_ML + 32 + (g >> 1) * 16 + n16];
        const v4f m0 = *(const v4f*)(ldf + L_ML + 4 * g), m1 = *(const v4f*)(ldf + L_ML + 16 + 4 * g);
        v4f X0[4], X1[4];
#pragma unroll
        for (int k = 0; k < 4; ++k) {
            X0[k] = *(const v4f*)(ldf + L_MB + (((g * 4 + k) * 2 + 0) * 64 + lam) * 4);
            X1[k] = *(const v4f*)(ldf + L_MB + (((g * 4 + k) * 2 + 1) * 64 + lam) * 4);
        }
        const float M0 = MM[0], M1 = MM[1];
        const float M = (g >> 1) ? M1 : M0;
        v4f S0 = {0.f, 0.f, 0.f, 0.f}, S1 = {0.f, 0.f, 0.f, 0.f};
#pragma unroll
        for (int k = 0; k < 4; ++k) {
            const float f0 = __builtin_amdgcn_exp2f(m0[k] - M0), f1 = __builtin_amdgcn_exp2f(m1[k] - M1);
            S0[0] = fmaf(f0, X0[k][0], S0[0]); S0[1] = fmaf(f1, X0[k][1], S0[1]); S0[2] = fmaf(f0, X0[k][2], S0[2]); S0[3] = fmaf(f1, X0[k][3], S0[3]);
            S1[0] = fmaf(f0, X1[k][0], S1[0]); S1[1] = fmaf(f1, X1[k][1], S1[1]); S1[2] = fmaf(f0, X1[k][2], S1[2]); S1[3] = fmaf(f1, X1[k][3], S1[3]);
        }
        const float L = sum_row16(lw * __builtin_amdgcn_exp2f(mo - M));
        const float inv = __builtin_amdgcn_rcpf(L);
        const float e0 = sum16x(tr32(S0[0], S0[1])) * inv, e1 = sum16x(tr32(S1[0], S1[1])) * inv;
        const float e2 = sum16x(tr32(S0[2], S0[3])) * inv, e3 = sum16x(tr32(S1[2], S1[3])) * inv;
        if (!bit4) {
            const v4u gv = {pkbf(e0, e1), tag, pkbf(e2, e3), tag};
            st_g(rs, opub, gv);
        } else if (wave == 0 && n16 == 0) {
            const v2u gs = {__float_as_uint(M + __builtin_amdgcn_logf(L)), tag};
            st_g2(rs, opub, gs);
        }
        __builtin_amdgcn_s_setprio(0);
    }
    v4u W4 = W0, W5 = W0, W6 = W0, W7 = W0;
    if constexpr (ROLE == 2) {
        const int f = rp0;
        const int wr = wave < 14 ? wave : 13;
        {
            const u16* p = w1 + (size_t)(wr * 32 + (lane >> 3)) * FF + f * 64 + (lane & 7) * 8;
            W0 = ld_nt(p); W1 = ld_nt(p + 8 * FF); W2 = ld_nt(p + 16 * FF); W3 = ld_nt(p + 24 * FF);
            const u16* q = w2 + (size_t)(f * 64) * DM + wr * 2048 + lane * 8;
            W4 = ld_nt(q); W5 = ld_nt(q + 512); W6 = ld_nt(q + 1024); W7 = ld_nt(q + 1536);
        }
        xres = *(const GAS u16*)(x + ((lane >> 3) & 1) * DM + wr * 32 + (lane & 7) * 4 + g);
        SCHED();
    }
    if constexpr (ROLE == 1) {
        const int h = rp0, cc = rp1, kk = wave >> 1, bb = wave & 1;
        const unsigned a0 = AG_OFF + ((((h * 2 + bb) * 16 + n16) * 8 + kk) * 8 + 2 * g) * 16;
        const unsigned af = ST_OFF + (h * 32 + (lane & 31)) * 128;
        BARRIER();
        v4u d0 = ld_g(rs, a0), d1 = ld_g(rs, a0 + 16);
        v2u st0 = ld_g2(rs, wave == 15 ? af : 0xFFFFFF00u);
        SCHED();
        if (wave >= 4 && wave < 12) {
            unsigned* w = lds + L_WT + ((wave - 4) * 32 + (lane >> 3)) * 32 + (lane & 7) * 4;
            *(v4u*)w = W0; *(v4u*)(w + 8 * 32) = W1; *(v4u*)(w + 16 * 32) = W2; *(v4u*)(w + 24 * 32) = W3;
            asm volatile("s_waitcnt lgkmcnt(0)" ::: "memory");
            if (lane == 0) __hip_atomic_fetch_add(lds + L_FL + 5, 1u, __ATOMIC_RELAXED, __HIP_MEMORY_SCOPE_WORKGROUP);
        }
        for (unsigned spins = 0; lds_ld_vol(lds + L_FL + 5) < 8u && spins < (SPIN_CAP << 8); ++spins) __builtin_amdgcn_s_sleep(1);
        CBAR();
        v4u B[4];
#pragma unroll
        for (int i = 0; i < 4; ++i) {
            const int lam = 8 * kk + 2 * g + (i >> 1), e = (2 * i) & 3;
            const int row = 128 * (e >> 1) + 8 * (lam & 15) + (lam >> 4);
            const v2u r0 = *(const v2u*)(lds + L_WT + row * 32 + 2 * n16), r1 = *(const v2u*)(lds + L_WT + (row + 4) * 32 + 2 * n16);
            B[0][i] = plo(r0[0], r1[0]); B[1][i] = phi(r0[0], r1[0]);
            B[2][i] = plo(r0[1], r1[1]); B[3][i] = phi(r0[1], r1[1]);
        }
        asm volatile("" : "+v"(B[0]), "+v"(B[1]), "+v"(B[2]), "+v"(B[3]));
        if (wave == 15) {
            for (unsigned spins = 0;; ++spins) {
                const bool ok = st0[1] == tag;
                if (__all(ok) || spins > SPIN_CAP) break;
                CBAR();
                const v2u t2 = ld_g2(rs, ok ? 0xFFFFFF00u : af);
                st0[0] = ok ? st0[0] : t2[0]; st0[1] = ok ? st0[1] : t2[1];
            }
            const float ls = __uint_as_float(st0[0]);
            const float fe = __builtin_amdgcn_exp2f(ls - max_row16(ls));
            const float fw = fe * __builtin_amdgcn_rcpf(sum_row16(fe));
            if (lane < 32) ldf[L_FS + lane] = fw;
            asm volatile("s_waitcnt lgkmcnt(0)" ::: "memory");
            if (lane == 0) __hip_atomic_store(lds + L_FL + 4, 1u, __ATOMIC_RELAXED, __HIP_MEMORY_SCOPE_WORKGROUP);
        }
        for (unsigned spins = 0;; ++spins) {
            const bool ok = tagok(d0, tag) && tagok(d1, tag);
            if (__all(ok) || spins > SPIN_CAP) break;
            __builtin_amdgcn_s_sleep(1);
            CBAR();
            repoll(rs, a0, d0, ok); repoll(rs, a0 + 16, d1, ok);
        }
        const v4u a = {d0[0], d0[2], d1[0], d1[2]};
        const v4f z = {0.f, 0.f, 0.f, 0.f};
        const v4f D0 = mfma(a, B[0], z), D1 = mfma(a, B[1], z), D2 = mfma(a, B[2], z), D3 = mfma(a, B[3], z);
        SCHED();
        for (unsigned spins = 0; lds_ld_vol(lds + L_FL + 4) == 0u && spins < (SPIN_CAP << 8); ++spins) {}
        CBAR();
        const v4f fv = *(const v4f*)(ldf + L_FS + bb * 16 + 4 * g);
        const float f0 = fv[0], f1 = fv[1], f2 = fv[2], f3 = fv[3];
        const float y0 = fmaf(f3, D0[3], fmaf(f2, D0[2], fmaf(f1, D0[1], f0 * D0[0])));
        const float y1 = fmaf(f3, D1[3], fmaf(f2, D1[2], fmaf(f1, D1[1], f0 * D1[0])));
        const float y2 = fmaf(f3, D2[3], fmaf(f2, D2[2], fmaf(f1, D2[1], f0 * D2[0])));
        const float y3 = fmaf(f3, D3[3], fmaf(f2, D3[2], fmaf(f1, D3[1], f0 * D3[0])));
        const float y = tr16(tr32(y0, y2), tr32(y1, y3));
        ldf[L_RP + wave * 64 + 4 * n16 + g] = y;
        BARRIER();
        if (wave == 0) {
            const int b2 = g & 1;
            v4f acc = {0.f, 0.f, 0.f, 0.f};
#pragma unroll
            for (int k = 0; k < 8; ++k) acc += *(const v4f*)(ldf + L_RP + (2 * k + b2) * 64 + 4 * n16);
            const v4u gv = {pkbf(acc[0], acc[1]), tag, pkbf(acc[2], acc[3]), tag};
            if (lane < 32) st_g(rs, YG_OFF + ((((h * 7 + cc) * 2 + (n16 >> 3)) * 2 + b2) * 8 + (n16 & 7)) * 16, gv);
        }
    } else if constexpr (ROLE == 2) {
        const int f = rp0;
        v4u B1[4], B2[4];
        {
            unsigned* w = wt + (lane >> 3) * 32 + (lane & 7) * 4;
            *(v4u*)w = W0; *(v4u*)(w + 272) = W1; *(v4u*)(w + 544) = W2; *(v4u*)(w + 816) = W3;
            CBAR();
            const unsigned* r = wt + g * 272 + 2 * n16;
#pragma unroll
            for (int i = 0; i < 4; ++i) {
                const v2u r0 = *(const v2u*)(r + 64 * i), r1 = *(const v2u*)(r + 64 * i + 32);
                B1[0][i] = plo(r0[0], r1[0]); B1[1][i] = phi(r0[0], r1[0]); B1[2][i] = plo(r0[1], r1[1]); B1[3][i] = phi(r0[1], r1[1]);
            }
            asm volatile("" : "+v"(B1[0]), "+v"(B1[1]), "+v"(B1[2]), "+v"(B1[3]));
        }
        BARRIER();
        if (wave < 14) {
            unsigned* w = lds + L_W2 + wave * 1024 + lane * 4;
            *(v4u*)w = W4; *(v4u*)(w + 256) = W5; *(v4u*)(w + 512) = W6; *(v4u*)(w + 768) = W7;
        }
        BARRIER();
        {
            const unsigned* r = lds + L_W2 + g * 8 * 224 + (wave < 14 ? wave : 13) * 16 + n16;
#pragma unroll
            for (int i = 0; i < 4; ++i) {
                const unsigned a0 = r[(2 * i) * 224], a1 = r[(2 * i + 1) * 224], c0 = r[(32 + 2 * i) * 224], c1 = r[(33 + 2 * i) * 224];
                B2[0][i] = plo(a0, a1); B2[1][i] = phi(a0, a1); B2[2][i] = plo(c0, c1); B2[3][i] = phi(c0, c1);
            }
            asm volatile("" : "+v"(B2[0]), "+v"(B2[1]), "+v"(B2[2]), "+v"(B2[3]), "+v"(xres));
        }
        const v4f z4 = {0.f, 0.f, 0.f, 0.f};
        unsigned klo = 0x00003f80u, khi = 0x3f800000u;
        asm volatile("" : "+v"(klo), "+v"(khi));
        if (wave < 14) {
            const unsigned ay = YG_OFF + (((g * 7 + (wave >> 1)) * 2 + (wave & 1)) * 16 + n16) * 16;
            constexpr unsigned HS = 4 * 7 * 512;
            v4u y[4];
#pragma unroll
            for (int i = 0; i < 4; ++i) y[i] = ld_g(rs, ay + i * HS);
            for (unsigned spins = 0;; ++spins) {
                const bool k0 = tagok(y[0], tag), k1 = tagok(y[1], tag), k2 = tagok(y[2], tag), k3 = tagok(y[3], tag);
                if (__all(k0 && k1 && k2 && k3) || spins > SPIN_CAP) break;
                __builtin_amdgcn_s_sleep(1);
                CBAR();
                repoll(rs, ay, y[0], k0); repoll(rs, ay + HS, y[1], k1); repoll(rs, ay + 2 * HS, y[2], k2); repoll(rs, ay + 3 * HS, y[3], k3);
            }
            float c0 = 0.f, c1 = 0.f, c2 = 0.f, c3 = 0.f;
#pragma unroll
            for (int i = 0; i < 4; ++i) {
                c0 = dot2(y[i][0], klo, c0); c1 = dot2(y[i][0], khi, c1);
                c2 = dot2(y[i][2], klo, c2); c3 = dot2(y[i][2], khi, c3);
            }
            const float ys = tr16(tr32(c0, c2), tr32(c1, c3));
            const unsigned hb16 = pkbf(bflo(xres) + rbf(ys), 0.f);
            ((u16*)(lds + L_HB))[(bit3 ? DM : 0) + wave * 32 + (lane & 7) * 4 + g] = (u16)hb16;
            CBAR();
            const v4u av = *(const v4u*)(lds + (n16 == 2 ? L_ONES : L_HB + (n16 & 1) * (DM / 2) + wave * 16 + g * 4));
            const v4f D0 = mfma(av, B1[0], z4), D1 = mfma(av, B1[1], z4), D2 = mfma(av, B1[2], z4), D3 = mfma(av, B1[3], z4);
            const v4f DS = mfma(av, av, z4);
            if (lane < 2) *(v2f*)(ldf + L_ST + (lane * 16 + wave) * 2) = (v2f){DS[2], lane ? DS[1] : DS[0]};
            if (lane < 16) {
                float* zp = ldf + L_RP + (wave * 64 + n16) * 4;
                *(v4f*)zp = D0; *(v4f*)(zp + 64) = D1; *(v4f*)(zp + 128) = D2; *(v4f*)(zp + 192) = D3;
            }
        }
        BARRIER();
        if (wave < 4) {
            const float* zp = ldf + L_RP + (wave * 16 + n16) * 4;
            const v2f sv = *(const v2f*)(n16 < 14 ? ldf + L_ST + ((g >> 1) * 16 + n16) * 2 : ldf + L_ZERO);
            v4f zs = *(const v4f*)(zp + g * 256) + *(const v4f*)(zp + (g + 4) * 256) + *(const v4f*)(zp + (g + 8) * 256);
            const v4f zl = *(const v4f*)(g < 2 ? zp + (g + 12) * 256 : ldf + L_ZERO);
            zs += zl;
            const float mean = sum_row16(sv[0]) * (1.f / DM);
            const float var = fmaf(-mean, mean, sum_row16(sv[1]) * (1.f / DM));
            float mb, idn;
            ln_params(mean, fmaxf(var, 0.f), mb, idn);
            const float zb = sum16x(tr32(zs[0], zs[1])), cs = sum16x(sum32x(zs[2]));
            const float zz = rbf((zb - mb * cs) * idn);
            const float act = zz * __builtin_amdgcn_rcpf(1.f + __builtin_amdgcn_exp2f(zz * -1.4426950408889634f));
            if (!bit4) ((u16*)(lds + L_AA))[(g >> 1) * 64 + 4 * n16 + wave] = (u16)pkbf(act, act);
        }
        BARRIER();
        if (wave < 14) {
            __builtin_amdgcn_s_setprio(3);
            const v4u a0 = *(const v4u*)(lds + L_AA + (n16 & 1) * 32 + g * 4), a1 = *(const v4u*)(lds + L_AA + (n16 & 1) * 32 + 16 + g * 4);
            const v4f D0 = mfma(a1, B2[2], mfma(a0, B2[0], z4)), D1 = mfma(a1, B2[3], mfma(a0, B2[1], z4));
            const v4u g0 = {pkbf(D0[0], D1[0]), tag, pkbf(D0[1], D1[1]), tag};
            if (lane < 16) st_g(rs, OG_OFF + (f * 224 + wave * 16 + n16) * 16, g0);
            __builtin_amdgcn_s_setprio(0);
        }
    } else if constexpr (ROLE == 3) {
        if (wave == 15) {
            const int f = rp0;
            unsigned klo = 0x00003f80u, khi = 0x3f800000u;
            asm volatile("" : "+v"(klo), "+v"(khi));
            const unsigned xr = *(const GAS unsigned*)(x + (lane >> 5) * DM + f * 16 + 2 * (lane & 7));
            const unsigned ay = YG_OFF + (((((lane >> 2) * 7 + (f >> 2)) * 2 + ((f >> 1) & 1)) * 2) * 8 + (f & 1) * 4 + (lane & 3)) * 16;
            v4u y0 = ld_g(rs, ay), y1 = ld_g(rs, ay + 128);
            for (unsigned spins = 0;; ++spins) {
                const bool k0 = tagok(y0, tag), k1 = tagok(y1, tag);
                if (__all(k0 && k1) || spins > SPIN_CAP) break;
                __builtin_amdgcn_s_sleep(8);
                CBAR();
                repoll(rs, ay, y0, k0); repoll(rs, ay + 128, y1, k1);
            }
            float e0 = tr16(tr32(bflo(y0[0]), bflo(y1[0])), tr32(bflo(y0[2]), bflo(y1[2])));
            float e1 = tr16(tr32(bfhi(y0[0]), bfhi(y1[0])), tr32(bfhi(y0[2]), bfhi(y1[2])));
            e0 += dpp<ROR4>(e0); e1 += dpp<ROR4>(e1); e0 += dpp<ROR8>(e0); e1 += dpp<ROR8>(e1);
            if ((lane & 12) == 0) *(v2f*)(ldf + L_RP + ((lane >> 5) * 8 + (lane & 3) * 2 + ((lane >> 4) & 1)) * 2) = (v2f){e0, e1};
            CBAR();
            const v2f yy = *(const v2f*)(ldf + L_RP + ((lane >> 5) * 8 + (lane & 7)) * 2);
            const unsigned hh2 = pkbf(bflo(xr) + rbf(yy[0]), bfhi(xr) + rbf(yy[1]));
            const unsigned a = OG_OFF + ((lane >> 3) * 224 + f * 8 + (lane & 7)) * 16;
            constexpr unsigned FS = 8 * 224 * 16;
            const int col = (lane >> 5) * DM + f * 16 + 2 * (lane & 7);
            v4u gv[4];
#pragma unroll
            for (int i = 0; i < 3; ++i) gv[i] = ld_g(rs, a + i * FS);
            const unsigned a3 = lane < 32 ? a + 3 * FS : a;
            gv[3] = ld_g(rs, a3);
            for (unsigned spins = 0;; ++spins) {
                const bool k0 = tagok(gv[0], tag), k1 = tagok(gv[1], tag), k2 = tagok(gv[2], tag), k3 = tagok(gv[3], tag);
                if (__all(k0 && k1 && k2 && k3) || spins > SPIN_CAP) break;
                CBAR();
                __builtin_amdgcn_s_sleep(1);
                repoll(rs, a, gv[0], k0); repoll(rs, a + FS, gv[1], k1); repoll(rs, a + 2 * FS, gv[2], k2); repoll(rs, a3, gv[3], k3);
            }
            if (lane >= 32) { gv[3][0] = 0u; gv[3][2] = 0u; }
            float c0 = 0.f, c1 = 0.f, c2 = 0.f, c3 = 0.f;
#pragma unroll
            for (int i = 0; i < 4; ++i) {
                c0 = dot2(gv[i][0], klo, c0); c1 = dot2(gv[i][0], khi, c1);
                c2 = dot2(gv[i][2], klo, c2); c3 = dot2(gv[i][2], khi, c3);
            }
            float s0 = sum16x(tr32(c0, c2)), s1 = sum16x(tr32(c1, c3));
            s0 += dpp<ROR8>(s0); s1 += dpp<ROR8>(s1);
            if ((lane & 24) == 0) {
                *(GAS unsigned*)(out + col) = pkbf(bflo(hh2) + rbf(s0), bfhi(hh2) + rbf(s1));
                if (f == 0 && lane == 0 ) __hip_atomic_store((GAS unsigned*)(ws + E_OFF), tag, __ATOMIC_RELAXED, __HIP_MEMORY_SCOPE_AGENT);
            }
        }
    }
}

__global__ void __launch_bounds__(NTHR, 4)
fused_decode(const u16* __restrict__ x, const u16* __restrict__ kc, const u16* __restrict__ vc, const u16* __restrict__ wq,
             const u16* __restrict__ wo, const u16* __restrict__ w1, const u16* __restrict__ w2, u16* __restrict__ out, char* ws) {
    __shared__ __attribute__((aligned(16))) unsigned lds[L_END];
    const int wave = __builtin_amdgcn_readfirstlane(threadIdx.x >> 6), lane = threadIdx.x & 63;
    const int kvh = blockIdx.x & 7, j = blockIdx.x >> 3;
    int role = 0, rp0 = 0, rp1 = 0;
    role_of(kvh, j, role, rp0, rp1);
    if (role == 1) body<1>(x, kc, vc, wq, wo, w1, w2, out, ws, lds, kvh, j, rp0, rp1, wave, lane);
    else if (role == 2) body<2>(x, kc, vc, wq, wo, w1, w2, out, ws, lds, kvh, j, rp0, rp1, wave, lane);
    else if (role == 3) body<3>(x, kc, vc, wq, wo, w1, w2, out, ws, lds, kvh, j, rp0, rp1, wave, lane);
    else body<0>(x, kc, vc, wq, wo, w1, w2, out, ws, lds, kvh, j, rp0, rp1, wave, lane);
}

extern "C" void kernel_launch(void* const* d_in, const int* in_sizes, int n_in, void* d_out, int out_size, void* d_ws,
                              size_t ws_size, hipStream_t stream) {
    fused_decode<<<256, NTHR, 0, stream>>>((const u16*)d_in[0], (const u16*)d_in[1], (const u16*)d_in[2], (const u16*)d_in[3],
                                           (const u16*)d_in[4], (const u16*)d_in[6], (const u16*)d_in[7], (u16*)d_out, (char*)d_ws);
}
```

```cpp
#define DIAG_F 3
#define DIAG_X 23.0f
#include <hip/hip_runtime.h>
#include <hip/hip_bf16.h>
#include <stdint.h>

typedef unsigned short u16;
typedef unsigned v4u __attribute__((ext_vector_type(4)));
typedef unsigned v2u __attribute__((ext_vector_type(2)));
typedef float v4f __attribute__((ext_vector_type(4)));
typedef float v2f __attribute__((ext_vector_type(2)));
typedef __bf16 bf16x2 __attribute__((ext_vector_type(2)));
typedef __bf16 bf16x8 __attribute__((ext_vector_type(8)));
#define GAS __attribute__((address_space(1)))

constexpr int DM = 448, NQ = 4096, SK = 4096, DH = 256, FF = 1792;
constexpr int NTHR = 1024;

constexpr unsigned E_OFF = 0;
constexpr unsigned QG_OFF = 4096;
constexpr unsigned AG_OFF = QG_OFF + 131072;
constexpr unsigned ST_OFF = AG_OFF + 524288;
constexpr unsigned YG_OFF = ST_OFF + 65536;
constexpr unsigned OG_OFF = YG_OFF + 57344;
constexpr unsigned WS_USED = OG_OFF + 100352;
constexpr int NF = 28;
constexpr unsigned SPIN_CAP = 1u << 17;

constexpr int L_QS = 0;
constexpr int L_ZERO = 260;
constexpr int L_ONES = 264;
constexpr int L_MF = 268;
constexpr int L_ML = 272;
constexpr int L_PA = L_ML + 64;
constexpr int L_QP = L_PA + 256;
constexpr int L_HB = L_QP + 448;
constexpr int L_ST = L_HB + 448;
constexpr int L_AA = L_ST + 128;
constexpr int L_XN = L_AA + 64;
constexpr int L_RP = L_XN + 448;
constexpr int L_FL = L_RP + 3584;
constexpr int L_FS = L_FL + 16;
constexpr int TILE_DW = 576;
constexpr int L_WT = L_FS + 32;
constexpr int L_MB = L_WT + 16 * 2 * TILE_DW;
constexpr int L_HK = L_MB + 8192;
constexpr int L_W2 = L_MB;
constexpr int L_END = L_MB + 14336;

__device__ __forceinline__ float bflo(unsigned v) { return __uint_as_float(v << 16); }
__device__ __forceinline__ float bfhi(unsigned v) { return __uint_as_float(v & 0xffff0000u); }
__device__ __forceinline__ unsigned pkbf(float lo, float hi) {
    bf16x2 r; r[0] = (__bf16)lo; r[1] = (__bf16)hi; return __builtin_bit_cast(unsigned, r);
}
__device__ __forceinline__ float rbf(float f) { return bflo(pkbf(f, f)); }
__device__ __forceinline__ float dot2(unsigned a, unsigned b, float acc) {
    return __builtin_amdgcn_fdot2_f32_bf16(__builtin_bit_cast(bf16x2, a), __builtin_bit_cast(bf16x2, b), acc, false);
}
__device__ __forceinline__ v4f mfma(v4u a, v4u b, v4f c) {
    return __builtin_amdgcn_mfma_f32_16x16x32_bf16(__builtin_bit_cast(bf16x8, a), __builtin_bit_cast(bf16x8, b), c, 0, 0, 0);
}
__device__ __forceinline__ unsigned plo(unsigned r0, unsigned r1) { return __builtin_amdgcn_perm(r1, r0, 0x05040100u); }
__device__ __forceinline__ unsigned phi(unsigned r0, unsigned r1) { return __builtin_amdgcn_perm(r1, r0, 0x07060302u); }
template <int CTRL> __device__ __forceinline__ float dpp(float x) {
    return __builtin_bit_cast(float, __builtin_amdgcn_mov_dpp(__builtin_bit_cast(int, x), CTRL, 0xf, 0xf, true));
}
constexpr int XOR1 = 0xB1, XOR2 = 0x4E, XOR7 = 0x141, ROR8 = 0x128, ROR4 = 0x124;
__device__ __forceinline__ float tr32(float a, float b) {
    auto s = __builtin_amdgcn_permlane32_swap(__float_as_uint(a), __float_as_uint(b), false, false);
    return __uint_as_float(s[0]) + __uint_as_float(s[1]);
}
__device__ __forceinline__ float tr16(float a, float b) {
    auto s = __builtin_amdgcn_permlane16_swap(__float_as_uint(a), __float_as_uint(b), false, false);
    return __uint_as_float(s[0]) + __uint_as_float(s[1]);
}
template <int CTRL> __device__ __forceinline__ float trdpp(float a, float b, bool bit) {
    const float keep = bit ? b : a, send = bit ? a : b;
    return keep + dpp<CTRL>(send);
}
__device__ __forceinline__ float sum32x(float x) { return tr32(x, x); }
__device__ __forceinline__ float sum16x(float x) { return tr16(x, x); }
__device__ __forceinline__ float sum_row16(float x) {
    x += dpp<XOR1>(x); x += dpp<XOR2>(x); x += dpp<XOR7>(x); x += dpp<ROR8>(x); return x;
}
__device__ __forceinline__ float max_row16(float x) {
    x = fmaxf(x, dpp<XOR1>(x)); x = fmaxf(x, dpp<XOR2>(x)); x = fmaxf(x, dpp<XOR7>(x)); x = fmaxf(x, dpp<ROR8>(x)); return x;
}
__device__ __forceinline__ float sum_wave(float x) { return sum32x(sum16x(sum_row16(x))); }

#define BARRIER() asm volatile("s_waitcnt lgkmcnt(0)\n\ts_barrier" ::: "memory")
#define CBAR() asm volatile("" ::: "memory")
#define SCHED() __builtin_amdgcn_sched_barrier(0)

__device__ __forceinline__ v4u ld_nt(const void* p) { return __builtin_nontemporal_load((const GAS v4u*)p); }
__device__ __forceinline__ v4u ld_g(__amdgpu_buffer_rsrc_t r, unsigned off) { return __builtin_amdgcn_raw_buffer_load_b128(r, off, 0, 16); }
__device__ __forceinline__ v2u ld_g2(__amdgpu_buffer_rsrc_t r, unsigned off) { return __builtin_amdgcn_raw_buffer_load_b64(r, off, 0, 16); }
__device__ __forceinline__ void st_g(__amdgpu_buffer_rsrc_t r, unsigned off, v4u v) { __builtin_amdgcn_raw_buffer_store_b128(v, r, off, 0, 16); }
__device__ __forceinline__ void st_g2(__amdgpu_buffer_rsrc_t r, unsigned off, v2u v) { __builtin_amdgcn_raw_buffer_store_b64(v, r, off, 0, 16); }
__device__ __forceinline__ bool tagok(v4u v, unsigned tag) { return v[1] == tag && v[3] == tag; }
__device__ __forceinline__ unsigned lds_ld_vol(unsigned* p) { return __hip_atomic_load(p, __ATOMIC_RELAXED, __HIP_MEMORY_SCOPE_WORKGROUP); }

__device__ __forceinline__ void ln_params(float mean, float var, float& mb, float& inv_den) {
    mb = rbf(mean);
    inv_den = __builtin_amdgcn_rcpf(rbf(__builtin_amdgcn_sqrtf(rbf(rbf(var) + 1e-5f))));
}
__device__ __forceinline__ void tile_frags(unsigned* t, int lane, v4u r0, v4u r1, v4u& f0, v4u& f1) {
    const int row = lane >> 2;
    unsigned* w = t + row * 16 + (row >> 3) * 16 + (lane & 3) * 4;
    *(v4u*)w = r0; *(v4u*)(w + 288) = r1;
    CBAR();
    const unsigned* r = t + (lane >> 4) * 144 + (lane & 15);
#pragma unroll
    for (int i = 0; i < 4; ++i) {
        const unsigned a = r[32 * i], c = r[32 * i + 16];
        f0[i] = plo(a, c); f1[i] = phi(a, c);
    }
}
__device__ __forceinline__ void repoll(__amdgpu_buffer_rsrc_t r, unsigned off, v4u& v, bool ok) {
    const v4u t = ld_g(r, ok ? 0xFFFFFF00u : off);
#pragma unroll
    for (int i = 0; i < 4; ++i) v[i] = ok ? v[i] : t[i];
}
constexpr int QX = 3;

__host__ __device__ __forceinline__ void role_of(int xl, int j, int& role, int& rp0, int& rp1) {
    role = 0; rp0 = 0; rp1 = 0;
    if (xl >= 6) { if (j < 14) { role = 2; rp0 = (xl - 6) * 14 + j; } }
    else if (j < 21) {
        const int gsv = (xl + 1 + j % 7) & 7, k = j / 7;
        const int tid = gsv < 6 ? k * 5 + (xl < gsv ? xl : xl - 1) : k * 6 + xl;
        if (tid < 14) { role = 1; rp0 = gsv * 2 + tid / 7; rp1 = tid % 7; }
    } else { const int u = (j - 21) * 6 + xl; if (u < NF) { role = 3; rp0 = u; } }
}

template <int ROLE>
__device__ __forceinline__ void body(const u16* __restrict__ x, const u16* __restrict__ kc, const u16* __restrict__ vc,
                                     const u16* __restrict__ wq, const u16* __restrict__ wo, const u16* __restrict__ w1,
                                     const u16* __restrict__ w2, u16* __restrict__ out, char* ws, unsigned* lds,
                                     const int kvh, const int j, const int rp0, const int rp1, const int wave, const int lane) {
    const __amdgpu_buffer_rsrc_t rs = __builtin_amdgcn_make_buffer_rsrc(ws, 0, WS_USED, 0x00020000);
    float* ldf = (float*)lds;
    const unsigned long long diag_t0 = __builtin_amdgcn_s_memrealtime();
    __builtin_amdgcn_s_setprio(3);
    const int b = j >> 4, s = j & 15;
    const int n16 = lane & 15, g = lane >> 4;
    const bool bit3 = (lane & 8) != 0, bit4 = (lane & 16) != 0;
    unsigned* wt = lds + L_WT + wave * (2 * TILE_DW);
    unsigned epoch = 0;
    v4u K[8], V[8];
    v4u W0 = {0, 0, 0, 0}, W1 = W0, W2 = W0, W3 = W0; unsigned xres = 0;
    unsigned stmp;
    {
        const size_t kvrow = (size_t)(b * 8 + kvh) * SK + s * 256;
        const unsigned ko = ((n16 & 7) * DH + ((n16 >> 3) * 4 + g) * 8) * 2, ko2 = ko + 4096;
        const unsigned vo = lane * 16, vo2 = vo + 4096;
        const u16* kb = kc + (kvrow + wave * 16) * DH;
        const u16* vb = vc + (kvrow + wave * 16) * DH;
#define KLD0 "global_load_dwordx4 %0, %[ko], %[kb] nt\n\t"
#define KLD1 "global_load_dwordx4 %1, %[ko], %[kb] offset:128 nt\n\t"
#define KLD2 "global_load_dwordx4 %2, %[ko], %[kb] offset:256 nt\n\t"
#define KLD3 "global_load_dwordx4 %3, %[ko], %[kb] offset:384 nt\n\t"
#define KV_LOADS_A KLD0 KLD1
#define KV_LOADS_A2 KLD2 KLD3
#define KV_LOADS_B \
            "global_load_dwordx4 %4, %[ko2], %[kb] nt\n\tglobal_load_dwordx4 %5, %[ko2], %[kb] offset:128 nt\n\t" \
            "global_load_dwordx4 %6, %[ko2], %[kb] offset:256 nt\n\tglobal_load_dwordx4 %7, %[ko2], %[kb] offset:384 nt\n\t" \
            "global_load_dwordx4 %8, %[vo], %[vb] nt\n\tglobal_load_dwordx4 %9, %[vo], %[vb] offset:1024 nt\n\t" \
            "global_load_dwordx4 %10, %[vo], %[vb] offset:2048 nt\n\tglobal_load_dwordx4 %11, %[vo], %[vb] offset:3072 nt\n\t" \
            "global_load_dwordx4 %12, %[vo2], %[vb] nt\n\tglobal_load_dwordx4 %13, %[vo2], %[vb] offset:1024 nt\n\t" \
            "global_load_dwordx4 %14, %[vo2], %[vb] offset:2048 nt\n\tglobal_load_dwordx4 %15, %[vo2], %[vb] offset:3072 nt\n\t"
#define KV_LOADS KV_LOADS_A KV_LOADS_A2 KV_LOADS_B
#define KV_OUT(c) c(K[0]), c(K[1]), c(K[2]), c(K[3]), c(K[4]), c(K[5]), c(K[6]), c(K[7]), c(V[0]), c(V[1]), c(V[2]), c(V[3]), c(V[4]), c(V[5]), c(V[6]), c(V[7])
        {
            const int i = (wave - 8) & 7;
            const u16* hk = kc + kvrow * DH + (i >> 2) * 8 * DH + (i & 3) * 64;
            const u16* hv = vc + kvrow * DH + i * 512;
            const unsigned lk = (unsigned)(size_t)(__attribute__((address_space(3))) unsigned*)(lds + L_HK + i * 256);
            unsigned keep;
            asm volatile("s_cmp_lt_u32 %[wv], 8\n\ts_cbranch_scc1 1f\n\t"
                         "s_setprio 1\n\t"
                         "s_mov_b32 %[keep], m0\n\ts_mov_b32 m0, %[lk]\n\ts_nop 0\n\tglobal_load_lds_dwordx4 %[ko], %[hk] nt\n\t"
                         "s_add_u32 m0, m0, 0x2000\n\ts_nop 0\n\tglobal_load_lds_dwordx4 %[vo], %[hv] nt\n\ts_mov_b32 m0, %[keep]\n\t"
                         KV_LOADS_A
                         "1:"
                         : KV_OUT("=&v"), [keep] "=&s"(keep)
                         : [wv] "s"(wave), [ko] "v"(ko), [ko2] "v"(ko2), [vo] "v"(vo), [vo2] "v"(vo2), [kb] "s"(kb), [vb] "s"(vb),
                           [hk] "s"(hk), [hv] "s"(hv), [lk] "s"(lk)
                         : "memory", "scc");
        }
        v4u xf = {0, 0, 0, 0}, xa = {0, 0, 0, 0};
        if (wave < 8) {
            const int lc = lane < 56 ? lane : 55;
            const u16* p0 = wave ? wq + (size_t)((j >> 4) * 224 + (wave - 1) * 32 + (lane >> 2)) * NQ + ((kvh + QX) & 7) * 512 + (j & 15) * 32 + (lane & 3) * 8 : x + lc * 8;
            const u16* p1 = wave ? p0 + (size_t)16 * NQ : p0 + DM;
            asm volatile("s_cmp_eq_u32 %[wv], 0\n\ts_cbranch_scc1 1f\n\t"
                         "global_load_dwordx4 %0, %3, off nt\n\tglobal_load_dwordx4 %1, %4, off nt\n\ts_branch 2f\n"
                         "1:\n\tglobal_load_dwordx4 %0, %3, off\n\tglobal_load_dwordx4 %1, %4, off\n"
                         "2:\n\tglobal_load_dword %2, %[zero], %[wsb] sc1"
                         : "=&v"(xf), "=&v"(xa), "+v"(epoch) : "v"(p0), "v"(p1), [wv] "s"(wave), [zero] "v"(0u), [wsb] "s"(ws) : "memory", "scc");
            if (wave == 0) {
                if (lane < 12) lds[L_ZERO + lane] = (lane >= 4 && lane < 8) ? 0x3f803f80u : ((lane == 8 || lane == 9) ? 0xff800000u : 0u);
                if (lane < 16) lds[L_FL + lane] = 0u;
            }
        }
        SCHED();
        asm volatile("s_waitcnt lgkmcnt(0)\n\ts_barrier" ::: "memory");
        asm volatile("s_cmp_lt_u32 %[wv], 8\n\ts_cbranch_scc1 1f\n\t"
                     KV_LOADS_A2
                     "global_load_dword %[ep], %[zero], %[wsb] sc1\n\t"
                     KV_LOADS_B
                     "1:"
                     : KV_OUT("+v"), [ep] "+v"(epoch)
                     : [wv] "s"(wave), [ko] "v"(ko), [ko2] "v"(ko2), [vo] "v"(vo), [vo2] "v"(vo2), [kb] "s"(kb), [vb] "s"(vb), [zero] "v"(0u), [wsb] "s"(ws)
                     : "memory", "scc");
        if (wave < 8) {
            asm volatile("s_waitcnt vmcnt(0)" : "+v"(xf), "+v"(xa), "+v"(epoch));
            const unsigned tag = epoch + 1u;
            if (wave) {
                v4u b0, b1;
                tile_frags(wt, lane, xf, xa, b0, b1);
                asm volatile("" : "+v"(b0), "+v"(b1));
                for (unsigned spins = 0; lds_ld_vol(lds + L_FL) == 0u && spins < (SPIN_CAP << 6); ++spins) __builtin_amdgcn_s_sleep(1);
                CBAR();
                const v4u a0 = *(const v4u*)(lds + L_XN + (n16 & 1) * (DM / 2) + (j >> 4) * 112 + (wave - 1) * 16 + g * 4);
                const v4f z = {0.f, 0.f, 0.f, 0.f};
                const v4f d0 = mfma(a0, b0, z), d1 = mfma(a0, b1, z);
                if (lane < 16) *(v4f*)(ldf + L_QP + ((wave - 1) * 16 + n16) * 4) = (v4f){d0[0], d1[0], d0[1], d1[1]};
                asm volatile("s_waitcnt lgkmcnt(0)" ::: "memory");
                if (lane == 0) __hip_atomic_fetch_add(lds + L_FL + 1, 1u, __ATOMIC_RELAXED, __HIP_MEMORY_SCOPE_WORKGROUP);
            } else {
                const float msk = lane < 56 ? 1.f : 0.f;
                float s0 = 0.f, s1 = 0.f;
#pragma unroll
                for (int k = 0; k < 4; ++k) { s0 += bflo(xf[k]) + bfhi(xf[k]); s1 += bflo(xa[k]) + bfhi(xa[k]); }
                s0 = sum_wave(s0 * msk) * (1.f / DM); s1 = sum_wave(s1 * msk) * (1.f / DM);
                float v0 = 0.f, v1 = 0.f;
#pragma unroll
                for (int k = 0; k < 4; ++k) {
                    float d;
                    d = bflo(xf[k]) - s0; v0 = fmaf(d, d, v0); d = bfhi(xf[k]) - s0; v0 = fmaf(d, d, v0);
                    d = bflo(xa[k]) - s1; v1 = fmaf(d, d, v1); d = bfhi(xa[k]) - s1; v1 = fmaf(d, d, v1);
                }
                v0 = sum_wave(v0 * msk) * (1.f / DM); v1 = sum_wave(v1 * msk) * (1.f / DM);
                float mb0, id0, mb1, id1;
                ln_params(s0, v0, mb0, id0); ln_params(s1, v1, mb1, id1);
                v4u n0, n1;
#pragma unroll
                for (int k = 0; k < 4; ++k) {
                    const unsigned c0 = pkbf(bflo(xf[k]) - mb0, bfhi(xf[k]) - mb0), c1 = pkbf(bflo(xa[k]) - mb1, bfhi(xa[k]) - mb1);
                    n0[k] = pkbf(bflo(c0) * id0, bfhi(c0) * id0);
                    n1[k] = pkbf(bflo(c1) * id1, bfhi(c1) * id1);
                }
                if (lane < 56) { *(v4u*)(lds + L_XN + lane * 4) = n0; *(v4u*)(lds + L_XN + DM / 2 + lane * 4) = n1; }
                asm volatile("s_waitcnt lgkmcnt(0)" ::: "memory");
                if (lane == 0) __hip_atomic_store(lds + L_FL, 1u, __ATOMIC_RELAXED, __HIP_MEMORY_SCOPE_WORKGROUP);
                for (unsigned spins = 0; lds_ld_vol(lds + L_FL + 1) < 7u && spins < (SPIN_CAP << 6); ++spins) {}
                CBAR();
                {
                    v2f t0 = {0.f, 0.f};
#pragma unroll
                    for (int w = 0; w < 7; ++w) t0 += *(const v2f*)(ldf + L_QP + (w * 16 + n16) * 4 + (g & 1) * 2);
                    const v4u gq = {__float_as_uint(t0[0]), tag, __float_as_uint(t0[1]), tag};
                    if (lane < 32) st_g(rs, QG_OFF + ((((kvh + QX) & 7) * 32 + j) * 32 + lane) * 16, gq);
                }
                const unsigned base = QG_OFF + ((kvh * 32 + g) * 32 + b * 16 + n16) * 16;
                v4u gg[8];
                __builtin_amdgcn_s_sleep(8);
                for (unsigned spins = 0;; ++spins) {
                    CBAR();
                    bool ok = true;
#pragma unroll
                    for (int i = 0; i < 8; ++i) gg[i] = ld_g(rs, base + i * 2048);
#pragma unroll
                    for (int i = 0; i < 8; ++i) ok &= tagok(gg[i], tag);
                    if (__all(ok) || spins > SPIN_CAP) break;
                }
#pragma unroll
                for (int m = 0; m < 4; ++m)
                    lds[L_QS + m * 64 + lane] = pkbf(__uint_as_float(gg[m][0]) + __uint_as_float(gg[4 + m][0]), __uint_as_float(gg[m][2]) + __uint_as_float(gg[4 + m][2]));
                asm volatile("s_waitcnt lgkmcnt(0)" ::: "memory");
                if (lane == 0) __hip_atomic_store(lds + L_FL + 2, 1u, __ATOMIC_RELAXED, __HIP_MEMORY_SCOPE_WORKGROUP);
                for (unsigned spins = 0; lds_ld_vol(lds + L_FL + 3) < 8u && spins < (SPIN_CAP << 6); ++spins) __builtin_amdgcn_s_sleep(2);
            }
            __builtin_amdgcn_s_waitcnt(0x0F70);
        }
        asm volatile("s_sub_u32 %[t], %[wv], 1\n\ts_cmp_gt_u32 %[t], 6\n\ts_cbranch_scc1 1f\n\t"
                     "s_setprio 1\n\t"
                     KV_LOADS
                     "1:\n\ts_setprio 0\n\t"
                     "s_cmp_lg_u32 %[wv], 0\n\ts_cbranch_scc1 2f\n\t"
                     "ds_read_b128 %0, %[hl]\n\tds_read_b128 %1, %[hl] offset:1024\n\tds_read_b128 %2, %[hl] offset:2048\n\tds_read_b128 %3, %[hl] offset:3072\n\t"
                     "ds_read_b128 %4, %[hl] offset:4096\n\tds_read_b128 %5, %[hl] offset:5120\n\tds_read_b128 %6, %[hl] offset:6144\n\tds_read_b128 %7, %[hl] offset:7168\n\t"
                     "ds_read_b128 %8, %[hl] offset:8192\n\tds_read_b128 %9, %[hl] offset:9216\n\tds_read_b128 %10, %[hl] offset:10240\n\tds_read_b128 %11, %[hl] offset:11264\n\t"
                     "ds_read_b128 %12, %[hl] offset:12288\n\tds_read_b128 %13, %[hl] offset:13312\n\tds_read_b128 %14, %[hl] offset:14336\n\tds_read_b128 %15, %[hl] offset:15360\n\t"
                     "s_waitcnt lgkmcnt(0)\n\t"
                     "2:"
                     : KV_OUT("+v"), [t] "=&s"(stmp)
                     : [wv] "s"(wave), [ko] "v"(ko), [ko2] "v"(ko2), [vo] "v"(vo), [vo2] "v"(vo2), [kb] "s"(kb), [vb] "s"(vb),
                       [hl] "v"((unsigned)(size_t)(__attribute__((address_space(3))) unsigned*)(lds + L_HK) + lane * 16)
                     : "memory", "scc");
        if (wave >= 8) {
            asm volatile("s_waitcnt vmcnt(17)" ::: "memory");
            if (lane == 0) __hip_atomic_fetch_add(lds + L_FL + 3, 1u, __ATOMIC_RELAXED, __HIP_MEMORY_SCOPE_WORKGROUP);
        }
        for (unsigned spins = 0; lds_ld_vol(lds + L_FL + 2) == 0u && spins < (SPIN_CAP << 6); ++spins) __builtin_amdgcn_s_sleep(2);
        CBAR();
    }
    float t;
    {
        const unsigned* qa = lds + L_QS + (n16 & 1) * 128 + (((n16 >> 1) & 1) * 4 + g) * 4;
        v4f d0 = {0.f, 0.f, 0.f, 0.f}, d1 = {0.f, 0.f, 0.f, 0.f};
        v4u qf[4];
#pragma unroll
        for (int i = 0; i < 4; ++i) qf[i] = *(const v4u*)(qa + i * 32);
        asm volatile("s_waitcnt vmcnt(8)" : "+v"(K[0]), "+v"(K[1]), "+v"(K[2]), "+v"(K[3]), "+v"(K[4]), "+v"(K[5]), "+v"(K[6]), "+v"(K[7]));
#pragma unroll
        for (int i = 0; i < 4; ++i) { d0 = mfma(qf[i], K[i], d0); d1 = mfma(qf[i], K[4 + i], d1); }
        const float s0 = trdpp<ROR8>(bit3 ? d0[2] : d0[0], bit3 ? d1[2] : d1[0], bit3);
        const float s1 = trdpp<ROR8>(bit3 ? d0[3] : d0[1], bit3 ? d1[3] : d1[1], bit3);
        t = rbf(bit4 ? s1 : s0) * (0.0625f * 1.4426950408889634f);
    }
    {
        const float mw = max_row16(t);
        const float p = __builtin_amdgcn_exp2f(t - mw);
        if (lane < 32) ((u16*)(lds + L_PA + wave * 16))[(g * 2 + (n16 & 1)) * 8 + (n16 >> 1)] = (u16)pkbf(p, p);
        CBAR();
        const float lw = sum_row16(p);
        if ((lane & 47) == 0) {
            ldf[L_ML + (lane >> 4) * 16 + wave] = mw; ldf[L_ML + 32 + (lane >> 4) * 16 + wave] = lw;
            __hip_atomic_fetch_max(ldf + L_MF + (lane >> 4), mw, __ATOMIC_RELAXED, __HIP_MEMORY_SCOPE_WORKGROUP);
        }
    }
    v4u PA[4];
#pragma unroll
    for (int tb = 0; tb < 4; ++tb) {
        const bool on = (n16 >> 2) == tb && ((n16 >> 1) & 1) == (g & 1);
        PA[tb] = *(const v4u*)(lds + (on ? L_PA + wave * 16 + ((n16 & 1) * 2 + (g >> 1)) * 4 : L_ZERO));
    }
    {
        v4f acc0 = {0.f, 0.f, 0.f, 0.f}, acc1 = {0.f, 0.f, 0.f, 0.f};
        asm volatile("s_waitcnt vmcnt(0)" : "+v"(V[0]), "+v"(V[1]), "+v"(V[2]), "+v"(V[3]), "+v"(V[4]), "+v"(V[5]), "+v"(V[6]), "+v"(V[7]), "+v"(epoch));
#pragma unroll
        for (int tt = 0; tt < 8; ++tt) {
            v4u bt;
#pragma unroll
            for (int ii = 0; ii < 4; ++ii)
                bt[ii] = (tt & 1) ? phi(V[2 * ii][tt >> 1], V[2 * ii + 1][tt >> 1]) : plo(V[2 * ii][tt >> 1], V[2 * ii + 1][tt >> 1]);
            if (tt < 4) acc0 = mfma(PA[tt & 3], bt, acc0); else acc1 = mfma(PA[tt & 3], bt, acc1);
        }
        *(v4f*)(ldf + L_MB + ((wave * 2 + 0) * 64 + lane) * 4) = acc0;
        *(v4f*)(ldf + L_MB + ((wave * 2 + 1) * 64 + lane) * 4) = acc1;
        asm volatile("s_waitcnt lgkmcnt(0)" ::: "memory");
        if (lane == 0) __hip_atomic_fetch_add(lds + L_MF + 2, 1u, __ATOMIC_RELAXED, __HIP_MEMORY_SCOPE_WORKGROUP);
    }
    const unsigned tag = epoch + 1u;
    if constexpr (ROLE == 1) {
        if (wave >= 4 && wave < 12) {
            const int h = rp0, cc = rp1;
            const u16* p = wo + (size_t)(h * 256 + (wave - 4) * 32 + (lane >> 3)) * DM + cc * 64 + (lane & 7) * 8;
            W0 = ld_nt(p); W1 = ld_nt(p + 8 * DM); W2 = ld_nt(p + 16 * DM); W3 = ld_nt(p + 24 * DM);
        }
    }
    SCHED();
    if (wave < 4) {
        __builtin_amdgcn_s_setprio(3);
        const int lam = wave * 16 + n16;
        unsigned opub = !bit4 ? AG_OFF + ((((kvh * 2 + (g >> 1)) * 2 + b) * 16 + s) * 64 + lam) * 16 : ST_OFF + (((kvh * 2 + (g >> 1)) * 2 + b) * 16 + s) * 128;
        asm volatile("" : "+v"(opub));
        for (unsigned spins = 0; lds_ld_vol(lds + L_MF + 2) < 16u && spins < (SPIN_CAP << 8); ++spins) {}
        CBAR();
        const v2f MM = *(const v2f*)(ldf + L_MF);
        const float mo = ldf[L_ML + (g >> 1) * 16 + n16];
        const float lw = ldf[L_ML + 32 + (g >> 1) * 16 + n16];
        const v4f m0 = *(const v4f*)(ldf + L_ML + 4 * g), m1 = *(const v4f*)(ldf + L_ML + 16 + 4 * g);
        v4f X0[4], X1[4];
#pragma unroll
        for (int k = 0; k < 4; ++k) {
            X0[k] = *(const v4f*)(ldf + L_MB + (((g * 4 + k) * 2 + 0) * 64 + lam) * 4);
            X1[k] = *(const v4f*)(ldf + L_MB + (((g * 4 + k) * 2 + 1) * 64 + lam) * 4);
        }
        const float M0 = MM[0], M1 = MM[1];
        const float M = (g >> 1) ? M1 : M0;
        v4f S0 = {0.f, 0.f, 0.f, 0.f}, S1 = {0.f, 0.f, 0.f, 0.f};
#pragma unroll
        for (int k = 0; k < 4; ++k) {
            const float f0 = __builtin_amdgcn_exp2f(m0[k] - M0), f1 = __builtin_amdgcn_exp2f(m1[k] - M1);
            S0[0] = fmaf(f0, X0[k][0], S0[0]); S0[1] = fmaf(f1, X0[k][1], S0[1]); S0[2] = fmaf(f0, X0[k][2], S0[2]); S0[3] = fmaf(f1, X0[k][3], S0[3]);
            S1[0] = fmaf(f0, X1[k][0], S1[0]); S1[1] = fmaf(f1, X1[k][1], S1[1]); S1[2] = fmaf(f0, X1[k][2], S1[2]); S1[3] = fmaf(f1, X1[k][3], S1[3]);
        }
        const float L = sum_row16(lw * __builtin_amdgcn_exp2f(mo - M));
        const float inv = __builtin_amdgcn_rcpf(L);
        const float e0 = sum16x(tr32(S0[0], S0[1])) * inv, e1 = sum16x(tr32(S1[0], S1[1])) * inv;
        const float e2 = sum16x(tr32(S0[2], S0[3])) * inv, e3 = sum16x(tr32(S1[2], S1[3])) * inv;
        if (!bit4) {
            const v4u gv = {pkbf(e0, e1), tag, pkbf(e2, e3), tag};
            st_g(rs, opub, gv);
        } else if (wave == 0 && n16 == 0) {
            const v2u gs = {__float_as_uint(M + __builtin_amdgcn_logf(L)), tag};
            st_g2(rs, opub, gs);
        }
        __builtin_amdgcn_s_setprio(0);
    }
    v4u W4 = W0, W5 = W0, W6 = W0, W7 = W0;
    if constexpr (ROLE == 2) {
        const int f = rp0;
        const int wr = wave < 14 ? wave : 13;
        {
            const u16* p = w1 + (size_t)(wr * 32 + (lane >> 3)) * FF + f * 64 + (lane & 7) * 8;
            W0 = ld_nt(p); W1 = ld_nt(p + 8 * FF); W2 = ld_nt(p + 16 * FF); W3 = ld_nt(p + 24 * FF);
            const u16* q = w2 + (size_t)(f * 64) * DM + wr * 2048 + lane * 8;
            W4 = ld_nt(q); W5 = ld_nt(q + 512); W6 = ld_nt(q + 1024); W7 = ld_nt(q + 1536);
        }
        xres = *(const GAS u16*)(x + ((lane >> 3) & 1) * DM + wr * 32 + (lane & 7) * 4 + g);
        SCHED();
    }
    if constexpr (ROLE == 1) {
        const int h = rp0, cc = rp1, kk = wave >> 1, bb = wave & 1;
        const unsigned a0 = AG_OFF + ((((h * 2 + bb) * 16 + n16) * 8 + kk) * 8 + 2 * g) * 16;
        const unsigned af = ST_OFF + (h * 32 + (lane & 31)) * 128;
        BARRIER();
        v4u d0 = ld_g(rs, a0), d1 = ld_g(rs, a0 + 16);
        v2u st0 = ld_g2(rs, wave == 15 ? af : 0xFFFFFF00u);
        SCHED();
        if (wave >= 4 && wave < 12) {
            unsigned* w = lds + L_WT + ((wave - 4) * 32 + (lane >> 3)) * 32 + (lane & 7) * 4;
            *(v4u*)w = W0; *(v4u*)(w + 8 * 32) = W1; *(v4u*)(w + 16 * 32) = W2; *(v4u*)(w + 24 * 32) = W3;
            asm volatile("s_waitcnt lgkmcnt(0)" ::: "memory");
            if (lane == 0) __hip_atomic_fetch_add(lds + L_FL + 5, 1u, __ATOMIC_RELAXED, __HIP_MEMORY_SCOPE_WORKGROUP);
        }
        for (unsigned spins = 0; lds_ld_vol(lds + L_FL + 5) < 8u && spins < (SPIN_CAP << 8); ++spins) __builtin_amdgcn_s_sleep(1);
        CBAR();
        v4u B[4];
#pragma unroll
        for (int i = 0; i < 4; ++i) {
            const int lam = 8 * kk + 2 * g + (i >> 1), e = (2 * i) & 3;
            const int row = 128 * (e >> 1) + 8 * (lam & 15) + (lam >> 4);
            const v2u r0 = *(const v2u*)(lds + L_WT + row * 32 + 2 * n16), r1 = *(const v2u*)(lds + L_WT + (row + 4) * 32 + 2 * n16);
            B[0][i] = plo(r0[0], r1[0]); B[1][i] = phi(r0[0], r1[0]);
            B[2][i] = plo(r0[1], r1[1]); B[3][i] = phi(r0[1], r1[1]);
        }
        asm volatile("" : "+v"(B[0]), "+v"(B[1]), "+v"(B[2]), "+v"(B[3]));
        if (wave == 15) {
            for (unsigned spins = 0;; ++spins) {
                const bool ok = st0[1] == tag;
                if (__all(ok) || spins > SPIN_CAP) break;
                CBAR();
                const v2u t2 = ld_g2(rs, ok ? 0xFFFFFF00u : af);
                st0[0] = ok ? st0[0] : t2[0]; st0[1] = ok ? st0[1] : t2[1];
            }
            const float ls = __uint_as_float(st0[0]);
            const float fe = __builtin_amdgcn_exp2f(ls - max_row16(ls));
            const float fw = fe * __builtin_amdgcn_rcpf(sum_row16(fe));
            if (lane < 32) ldf[L_FS + lane] = fw;
            asm volatile("s_waitcnt lgkmcnt(0)" ::: "memory");
            if (lane == 0) __hip_atomic_store(lds + L_FL + 4, 1u, __ATOMIC_RELAXED, __HIP_MEMORY_SCOPE_WORKGROUP);
        }
        for (unsigned spins = 0;; ++spins) {
            const bool ok = tagok(d0, tag) && tagok(d1, tag);
            if (__all(ok) || spins > SPIN_CAP) break;
            __builtin_amdgcn_s_sleep(1);
            CBAR();
            repoll(rs, a0, d0, ok); repoll(rs, a0 + 16, d1, ok);
        }
        const v4u a = {d0[0], d0[2], d1[0], d1[2]};
        const v4f z = {0.f, 0.f, 0.f, 0.f};
        const v4f D0 = mfma(a, B[0], z), D1 = mfma(a, B[1], z), D2 = mfma(a, B[2], z), D3 = mfma(a, B[3], z);
        SCHED();
        for (unsigned spins = 0; lds_ld_vol(lds + L_FL + 4) == 0u && spins < (SPIN_CAP << 8); ++spins) {}
        CBAR();
        const v4f fv = *(const v4f*)(ldf + L_FS + bb * 16 + 4 * g);
        const float f0 = fv[0], f1 = fv[1], f2 = fv[2], f3 = fv[3];
        const float y0 = fmaf(f3, D0[3], fmaf(f2, D0[2], fmaf(f1, D0[1], f0 * D0[0])));
        const float y1 = fmaf(f3, D1[3], fmaf(f2, D1[2], fmaf(f1, D1[1], f0 * D1[0])));
        const float y2 = fmaf(f3, D2[3], fmaf(f2, D2[2], fmaf(f1, D2[1], f0 * D2[0])));
        const float y3 = fmaf(f3, D3[3], fmaf(f2, D3[2], fmaf(f1, D3[1], f0 * D3[0])));
        const float y = tr16(tr32(y0, y2), tr32(y1, y3));
        ldf[L_RP + wave * 64 + 4 * n16 + g] = y;
        BARRIER();
        if (wave == 0) {
            const int b2 = g & 1;
            v4f acc = {0.f, 0.f, 0.f, 0.f};
#pragma unroll
            for (int k = 0; k < 8; ++k) acc += *(const v4f*)(ldf + L_RP + (2 * k + b2) * 64 + 4 * n16);
            const v4u gv = {pkbf(acc[0], acc[1]), tag, pkbf(acc[2], acc[3]), tag};
            if (lane < 32) st_g(rs, YG_OFF + ((((h * 7 + cc) * 2 + (n16 >> 3)) * 2 + b2) * 8 + (n16 & 7)) * 16, gv);
        }
    } else if constexpr (ROLE == 2) {
        const int f = rp0;
        v4u B1[4], B2[4];
        {
            unsigned* w = wt + (lane >> 3) * 32 + (lane & 7) * 4;
            *(v4u*)w = W0; *(v4u*)(w + 272) = W1; *(v4u*)(w + 544) = W2; *(v4u*)(w + 816) = W3;
            CBAR();
            const unsigned* r = wt + g * 272 + 2 * n16;
#pragma unroll
            for (int i = 0; i < 4; ++i) {
                const v2u r0 = *(const v2u*)(r + 64 * i), r1 = *(const v2u*)(r + 64 * i + 32);
                B1[0][i] = plo(r0[0], r1[0]); B1[1][i] = phi(r0[0], r1[0]); B1[2][i] = plo(r0[1], r1[1]); B1[3][i] = phi(r0[1], r1[1]);
            }
            asm volatile("" : "+v"(B1[0]), "+v"(B1[1]), "+v"(B1[2]), "+v"(B1[3]));
        }
        BARRIER();
        if (wave < 14) {
            unsigned* w = lds + L_W2 + wave * 1024 + lane * 4;
            *(v4u*)w = W4; *(v4u*)(w + 256) = W5; *(v4u*)(w + 512) = W6; *(v4u*)(w + 768) = W7;
        }
        BARRIER();
        {
            const unsigned* r = lds + L_W2 + g * 8 * 224 + (wave < 14 ? wave : 13) * 16 + n16;
#pragma unroll
            for (int i = 0; i < 4; ++i) {
                const unsigned a0 = r[(2 * i) * 224], a1 = r[(2 * i + 1) * 224], c0 = r[(32 + 2 * i) * 224], c1 = r[(33 + 2 * i) * 224];
                B2[0][i] = plo(a0, a1); B2[1][i] = phi(a0, a1); B2[2][i] = plo(c0, c1); B2[3][i] = phi(c0, c1);
            }
            asm volatile("" : "+v"(B2[0]), "+v"(B2[1]), "+v"(B2[2]), "+v"(B2[3]), "+v"(xres));
        }
        const v4f z4 = {0.f, 0.f, 0.f, 0.f};
        unsigned klo = 0x00003f80u, khi = 0x3f800000u;
        asm volatile("" : "+v"(klo), "+v"(khi));
        if (wave < 14) {
            const unsigned ay = YG_OFF + (((g * 7 + (wave >> 1)) * 2 + (wave & 1)) * 16 + n16) * 16;
            constexpr unsigned HS = 4 * 7 * 512;
            v4u y[4];
#pragma unroll
            for (int i = 0; i < 4; ++i) y[i] = ld_g(rs, ay + i * HS);
            for (unsigned spins = 0;; ++spins) {
                const bool k0 = tagok(y[0], tag), k1 = tagok(y[1], tag), k2 = tagok(y[2], tag), k3 = tagok(y[3], tag);
                if (__all(k0 && k1 && k2 && k3) || spins > SPIN_CAP) break;
                __builtin_amdgcn_s_sleep(1);
                CBAR();
                repoll(rs, ay, y[0], k0); repoll(rs, ay + HS, y[1], k1); repoll(rs, ay + 2 * HS, y[2], k2); repoll(rs, ay + 3 * HS, y[3], k3);
            }
            float c0 = 0.f, c1 = 0.f, c2 = 0.f, c3 = 0.f;
#pragma unroll
            for (int i = 0; i < 4; ++i) {
                c0 = dot2(y[i][0], klo, c0); c1 = dot2(y[i][0], khi, c1);
                c2 = dot2(y[i][2], klo, c2); c3 = dot2(y[i][2], khi, c3);
            }
            const float ys = tr16(tr32(c0, c2), tr32(c1, c3));
            const unsigned hb16 = pkbf(bflo(xres) + rbf(ys), 0.f);
            ((u16*)(lds + L_HB))[(bit3 ? DM : 0) + wave * 32 + (lane & 7) * 4 + g] = (u16)hb16;
            CBAR();
            const v4u av = *(const v4u*)(lds + (n16 == 2 ? L_ONES : L_HB + (n16 & 1) * (DM / 2) + wave * 16 + g * 4));
            const v4f D0 = mfma(av, B1[0], z4), D1 = mfma(av, B1[1], z4), D2 = mfma(av, B1[2], z4), D3 = mfma(av, B1[3], z4);
            const v4f DS = mfma(av, av, z4);
            if (lane < 2) *(v2f*)(ldf + L_ST + (lane * 16 + wave) * 2) = (v2f){DS[2], lane ? DS[1] : DS[0]};
            if (lane < 16) {
                float* zp = ldf + L_RP + (wave * 64 + n16) * 4;
                *(v4f*)zp = D0; *(v4f*)(zp + 64) = D1; *(v4f*)(zp + 128) = D2; *(v4f*)(zp + 192) = D3;
            }
        }
        BARRIER();
        if (wave < 4) {
            const float* zp = ldf + L_RP + (wave * 16 + n16) * 4;
            const v2f sv = *(const v2f*)(n16 < 14 ? ldf + L_ST + ((g >> 1) * 16 + n16) * 2 : ldf + L_ZERO);
            v4f zs = *(const v4f*)(zp + g * 256) + *(const v4f*)(zp + (g + 4) * 256) + *(const v4f*)(zp + (g + 8) * 256);
            const v4f zl = *(const v4f*)(g < 2 ? zp + (g + 12) * 256 : ldf + L_ZERO);
            zs += zl;
            const float mean = sum_row16(sv[0]) * (1.f / DM);
            const float var = fmaf(-mean, mean, sum_row16(sv[1]) * (1.f / DM));
            float mb, idn;
            ln_params(mean, fmaxf(var, 0.f), mb, idn);
            const float zb = sum16x(tr32(zs[0], zs[1])), cs = sum16x(sum32x(zs[2]));
            const float zz = rbf((zb - mb * cs) * idn);
            const float act = zz * __builtin_amdgcn_rcpf(1.f + __builtin_amdgcn_exp2f(zz * -1.4426950408889634f));
            if (!bit4) ((u16*)(lds + L_AA))[(g >> 1) * 64 + 4 * n16 + wave] = (u16)pkbf(act, act);
        }
        BARRIER();
        if (wave < 14) {
            __builtin_amdgcn_s_setprio(3);
            const v4u a0 = *(const v4u*)(lds + L_AA + (n16 & 1) * 32 + g * 4), a1 = *(const v4u*)(lds + L_AA + (n16 & 1) * 32 + 16 + g * 4);
            const v4f D0 = mfma(a1, B2[2], mfma(a0, B2[0], z4)), D1 = mfma(a1, B2[3], mfma(a0, B2[1], z4));
            const v4u g0 = {pkbf(D0[0], D1[0]), tag, pkbf(D0[1], D1[1]), tag};
            if (lane < 16) st_g(rs, OG_OFF + (f * 224 + wave * 16 + n16) * 16, g0);
            __builtin_amdgcn_s_setprio(0);
        }
    } else if constexpr (ROLE == 3) {
        if (wave == 15) {
            const int f = rp0;
            unsigned klo = 0x00003f80u, khi = 0x3f800000u;
            asm volatile("" : "+v"(klo), "+v"(khi));
            const unsigned xr = *(const GAS unsigned*)(x + (lane >> 5) * DM + f * 16 + 2 * (lane & 7));
            const unsigned ay = YG_OFF + (((((lane >> 2) * 7 + (f >> 2)) * 2 + ((f >> 1) & 1)) * 2) * 8 + (f & 1) * 4 + (lane & 3)) * 16;
            v4u y0 = ld_g(rs, ay), y1 = ld_g(rs, ay + 128);
            for (unsigned spins = 0;; ++spins) {
                const bool k0 = tagok(y0, tag), k1 = tagok(y1, tag);
                if (__all(k0 && k1) || spins > SPIN_CAP) break;
                __builtin_amdgcn_s_sleep(8);
                CBAR();
                repoll(rs, ay, y0, k0); repoll(rs, ay + 128, y1, k1);
            }
            float e0 = tr16(tr32(bflo(y0[0]), bflo(y1[0])), tr32(bflo(y0[2]), bflo(y1[2])));
            float e1 = tr16(tr32(bfhi(y0[0]), bfhi(y1[0])), tr32(bfhi(y0[2]), bfhi(y1[2])));
            e0 += dpp<ROR4>(e0); e1 += dpp<ROR4>(e1); e0 += dpp<ROR8>(e0); e1 += dpp<ROR8>(e1);
            if ((lane & 12) == 0) *(v2f*)(ldf + L_RP + ((lane >> 5) * 8 + (lane & 3) * 2 + ((lane >> 4) & 1)) * 2) = (v2f){e0, e1};
            CBAR();
            const v2f yy = *(const v2f*)(ldf + L_RP + ((lane >> 5) * 8 + (lane & 7)) * 2);
            const unsigned hh2 = pkbf(bflo(xr) + rbf(yy[0]), bfhi(xr) + rbf(yy[1]));
            const unsigned a = OG_OFF + ((lane >> 3) * 224 + f * 8 + (lane & 7)) * 16;
            constexpr unsigned FS = 8 * 224 * 16;
            const int col = (lane >> 5) * DM + f * 16 + 2 * (lane & 7);
            v4u gv[4];
#pragma unroll
            for (int i = 0; i < 3; ++i) gv[i] = ld_g(rs, a + i * FS);
            const unsigned a3 = lane < 32 ? a + 3 * FS : a;
            gv[3] = ld_g(rs, a3);
            for (unsigned spins = 0;; ++spins) {
                const bool k0 = tagok(gv[0], tag), k1 = tagok(gv[1], tag), k2 = tagok(gv[2], tag), k3 = tagok(gv[3], tag);
                if (__all(k0 && k1 && k2 && k3) || spins > SPIN_CAP) break;
                CBAR();
                __builtin_amdgcn_s_sleep(1);
                repoll(rs, a, gv[0], k0); repoll(rs, a + FS, gv[1], k1); repoll(rs, a + 2 * FS, gv[2], k2); repoll(rs, a3, gv[3], k3);
            }
            if (lane >= 32) { gv[3][0] = 0u; gv[3][2] = 0u; }
            float c0 = 0.f, c1 = 0.f, c2 = 0.f, c3 = 0.f;
#pragma unroll
            for (int i = 0; i < 4; ++i) {
                c0 = dot2(gv[i][0], klo, c0); c1 = dot2(gv[i][0], khi, c1);
                c2 = dot2(gv[i][2], klo, c2); c3 = dot2(gv[i][2], khi, c3);
            }
            float s0 = sum16x(tr32(c0, c2)), s1 = sum16x(tr32(c1, c3));
            s0 += dpp<ROR8>(s0); s1 += dpp<ROR8>(s1);
            if ((lane & 24) == 0) {
                const float du = (float)(unsigned)(__builtin_amdgcn_s_memrealtime() - diag_t0) * 0.01f;
                const float dg = (f == DIAG_F && lane == 0 && du > DIAG_X) ? 64.f + du * 4.f : 0.f;
                *(GAS unsigned*)(out + col) = pkbf(bflo(hh2) + rbf(s0) + dg, bfhi(hh2) + rbf(s1));
                if (f == 0 && lane == 0 ) __hip_atomic_store((GAS unsigned*)(ws + E_OFF), tag, __ATOMIC_RELAXED, __HIP_MEMORY_SCOPE_AGENT);
            }
        }
    }
}

__global__ void __launch_bounds__(NTHR, 4)
fused_decode(const u16* __restrict__ x, const u16* __restrict__ kc, const u16* __restrict__ vc, const u16* __restrict__ wq,
             const u16* __restrict__ wo, const u16* __restrict__ w1, const u16* __restrict__ w2, u16* __restrict__ out, char* ws) {
    __shared__ __attribute__((aligned(16))) unsigned lds[L_END];
    const int wave = __builtin_amdgcn_readfirstlane(threadIdx.x >> 6), lane = threadIdx.x & 63;
    const int kvh = blockIdx.x & 7, j = blockIdx.x >> 3;
    int role = 0, rp0 = 0, rp1 = 0;
    role_of(kvh, j, role, rp0, rp1);
    if (role == 1) body<1>(x, kc, vc, wq, wo, w1, w2, out, ws, lds, kvh, j, rp0, rp1, wave, lane);
    else if (role == 2) body<2>(x, kc, vc, wq, wo, w1, w2, out, ws, lds, kvh, j, rp0, rp1, wave, lane);
    else if (role == 3) body<3>(x, kc, vc, wq, wo, w1, w2, out, ws, lds, kvh, j, rp0, rp1, wave, lane);
    else body<0>(x, kc, vc, wq, wo, w1, w2, out, ws, lds, kvh, j, rp0, rp1, wave, lane);
}

extern "C" void kernel_launch(void* const* d_in, const int* in_sizes, int n_in, void* d_out, int out_size, void* d_ws,
                              size_t ws_size, hipStream_t stream) {
    fused_decode<<<256, NTHR, 0, stream>>>((const u16*)d_in[0], (const u16*)d_in[1], (const u16*)d_in[2], (const u16*)d_in[3],
                                           (const u16*)d_in[4], (const u16*)d_in[6], (const u16*)d_in[7], (u16*)d_out, (char*)d_ws);
}
```

```cpp
#include <hip/hip_runtime.h>
#include <hip/hip_bf16.h>
#include <stdint.h>

typedef unsigned short u16;
typedef unsigned v4u __attribute__((ext_vector_type(4)));
typedef unsigned v2u __attribute__((ext_vector_type(2)));
typedef float v4f __attribute__((ext_vector_type(4)));
typedef float v2f __attribute__((ext_vector_type(2)));
typedef __bf16 bf16x2 __attribute__((ext_vector_type(2)));
typedef __bf16 bf16x8 __attribute__((ext_vector_type(8)));
#define GAS __attribute__((address_space(1)))

constexpr int DM = 448, NQ = 4096, SK = 4096, DH = 256, FF = 1792;
constexpr int NTHR = 1024;

constexpr unsigned E_OFF = 0;
constexpr unsigned QG_OFF = 4096;
constexpr unsigned AG_OFF = QG_OFF + 131072;
constexpr unsigned ST_OFF = AG_OFF + 524288;
constexpr unsigned YG_OFF = ST_OFF + 65536;
constexpr unsigned OG_OFF = YG_OFF + 57344;
constexpr unsigned WS_USED = OG_OFF + 103936;
constexpr int NF = 28;
constexpr unsigned SPIN_CAP = 1u << 17;

constexpr int L_QS = 0;
constexpr int L_ZERO = 260;
constexpr int L_ONES = 264;
constexpr int L_MF = 268;
constexpr int L_ML = 272;
constexpr int L_PA = L_ML + 64;
constexpr int L_QP = L_PA + 256;
constexpr int L_HB = L_QP + 448;
constexpr int L_ST = L_HB + 448;
constexpr int L_AA = L_ST + 128;
constexpr int L_XN = L_AA + 64;
constexpr int L_RP = L_XN + 448;
constexpr int L_FL = L_RP + 3584;
constexpr int L_FS = L_FL + 16;
constexpr int TILE_DW = 576;
constexpr int L_WT = L_FS + 32;
constexpr int L_MB = L_WT + 16 * 2 * TILE_DW;
constexpr int L_HK = L_MB + 8192;
constexpr int L_W2 = L_MB;
constexpr int L_END = L_MB + 14336;

__device__ __forceinline__ float bflo(unsigned v) { return __uint_as_float(v << 16); }
__device__ __forceinline__ float bfhi(unsigned v) { return __uint_as_float(v & 0xffff0000u); }
__device__ __forceinline__ unsigned pkbf(float lo, float hi) {
    bf16x2 r; r[0] = (__bf16)lo; r[1] = (__bf16)hi; return __builtin_bit_cast(unsigned, r);
}
__device__ __forceinline__ float rbf(float f) { return bflo(pkbf(f, f)); }
__device__ __forceinline__ float dot2(unsigned a, unsigned b, float acc) {
    return __builtin_amdgcn_fdot2_f32_bf16(__builtin_bit_cast(bf16x2, a), __builtin_bit_cast(bf16x2, b), acc, false);
}
__device__ __forceinline__ v4f mfma(v4u a, v4u b, v4f c) {
    return __builtin_amdgcn_mfma_f32_16x16x32_bf16(__builtin_bit_cast(bf16x8, a), __builtin_bit_cast(bf16x8, b), c, 0, 0, 0);
}
__device__ __forceinline__ unsigned plo(unsigned r0, unsigned r1) { return __builtin_amdgcn_perm(r1, r0, 0x05040100u); }
__device__ __forceinline__ unsigned phi(unsigned r0, unsigned r1) { return __builtin_amdgcn_perm(r1, r0, 0x07060302u); }
template <int CTRL> __device__ __forceinline__ float dpp(float x) {
    return __builtin_bit_cast(float, __builtin_amdgcn_mov_dpp(__builtin_bit_cast(int, x), CTRL, 0xf, 0xf, true));
}
constexpr int XOR1 = 0xB1, XOR2 = 0x4E, XOR7 = 0x141, ROR8 = 0x128, ROR4 = 0x124;
__device__ __forceinline__ float tr32(float a, float b) {
    auto s = __builtin_amdgcn_permlane32_swap(__float_as_uint(a), __float_as_uint(b), false, false);
    return __uint_as_float(s[0]) + __uint_as_float(s[1]);
}
__device__ __forceinline__ float tr16(float a, float b) {
    auto s = __builtin_amdgcn_permlane16_swap(__float_as_uint(a), __float_as_uint(b), false, false);
    return __uint_as_float(s[0]) + __uint_as_float(s[1]);
}
template <int CTRL> __device__ __forceinline__ float trdpp(float a, float b, bool bit) {
    const float keep = bit ? b : a, send = bit ? a : b;
    return keep + dpp<CTRL>(send);
}
__device__ __forceinline__ float sum32x(float x) { return tr32(x, x); }
__device__ __forceinline__ float sum16x(float x) { return tr16(x, x); }
__device__ __forceinline__ float sum_row16(float x) {
    x += dpp<XOR1>(x); x += dpp<XOR2>(x); x += dpp<XOR7>(x); x += dpp<ROR8>(x); return x;
}
__device__ __forceinline__ float max_row16(float x) {
    x = fmaxf(x, dpp<XOR1>(x)); x = fmaxf(x, dpp<XOR2>(x)); x = fmaxf(x, dpp<XOR7>(x)); x = fmaxf(x, dpp<ROR8>(x)); return x;
}
__device__ __forceinline__ float sum_wave(float x) { return sum32x(sum16x(sum_row16(x))); }

#define BARRIER() asm volatile("s_waitcnt lgkmcnt(0)\n\ts_barrier" ::: "memory")
#define CBAR() asm volatile("" ::: "memory")
#define SCHED() __builtin_amdgcn_sched_barrier(0)

__device__ __forceinline__ v4u ld_nt(const void* p) { return __builtin_nontemporal_load((const GAS v4u*)p); }
__device__ __forceinline__ v4u ld_g(__amdgpu_buffer_rsrc_t r, unsigned off) { return __builtin_amdgcn_raw_buffer_load_b128(r, off, 0, 16); }
__device__ __forceinline__ v2u ld_g2(__amdgpu_buffer_rsrc_t r, unsigned off) { return __builtin_amdgcn_raw_buffer_load_b64(r, off, 0, 16); }
__device__ __forceinline__ void st_g(__amdgpu_buffer_rsrc_t r, unsigned off, v4u v) { __builtin_amdgcn_raw_buffer_store_b128(v, r, off, 0, 16); }
__device__ __forceinline__ void st_g2(__amdgpu_buffer_rsrc_t r, unsigned off, v2u v) { __builtin_amdgcn_raw_buffer_store_b64(v, r, off, 0, 16); }
__device__ __forceinline__ bool tagok(v4u v, unsigned tag) { return v[1] == tag && v[3] == tag; }
__device__ __forceinline__ unsigned lds_ld_vol(unsigned* p) { return __hip_atomic_load(p, __ATOMIC_RELAXED, __HIP_MEMORY_SCOPE_WORKGROUP); }

__device__ __forceinline__ void ln_params(float mean, float var, float& mb, float& inv_den) {
    mb = rbf(mean);
    inv_den = __builtin_amdgcn_rcpf(rbf(__builtin_amdgcn_sqrtf(rbf(rbf(var) + 1e-5f))));
}
__device__ __forceinline__ void tile_frags(unsigned* t, int lane, v4u r0, v4u r1, v4u& f0, v4u& f1) {
    const int row = lane >> 2;
    unsigned* w = t + row * 16 + (row >> 3) * 16 + (lane & 3) * 4;
    *(v4u*)w = r0; *(v4u*)(w + 288) = r1;
    CBAR();
    const unsigned* r = t + (lane >> 4) * 144 + (lane & 15);
#pragma unroll
    for (int i = 0; i < 4; ++i) {
        const unsigned a = r[32 * i], c = r[32 * i + 16];
        f0[i] = plo(a, c); f1[i] = phi(a, c);
    }
}
__device__ __forceinline__ void repoll(__amdgpu_buffer_rsrc_t r, unsigned off, v4u& v, bool ok) {
    const v4u t = ld_g(r, ok ? 0xFFFFFF00u : off);
#pragma unroll
    for (int i = 0; i < 4; ++i) v[i] = ok ? v[i] : t[i];
}
constexpr int QX = 3;

__host__ __device__ __forceinline__ void role_of(int xl, int j, int& role, int& rp0, int& rp1) {
    role = 0; rp0 = 0; rp1 = 0;
    if (xl >= 6) { if (j < 14) { role = 2; rp0 = (xl - 6) * 14 + j; } }
    else if (j < 21) {
        const int gsv = (xl + 1 + j % 7) & 7, k = j / 7;
        const int tid = gsv < 6 ? k * 5 + (xl < gsv ? xl : xl - 1) : k * 6 + xl;
        if (tid < 14) { role = 1; rp0 = gsv * 2 + tid / 7; rp1 = tid % 7; }
    } else { const int u = (j - 21) * 6 + xl; if (u < NF) { role = 3; rp0 = u; } }
}

template <int ROLE>
__device__ __forceinline__ void body(const u16* __restrict__ x, const u16* __restrict__ kc, const u16* __restrict__ vc,
                                     const u16* __restrict__ wq, const u16* __restrict__ wo, const u16* __restrict__ w1,
                                     const u16* __restrict__ w2, u16* __restrict__ out, char* ws, unsigned* lds,
                                     const int kvh, const int j, const int rp0, const int rp1, const int wave, const int lane) {
    const __amdgpu_buffer_rsrc_t rs = __builtin_amdgcn_make_buffer_rsrc(ws, 0, WS_USED, 0x00020000);
    float* ldf = (float*)lds;
    __builtin_amdgcn_s_setprio(3);
    const int b = j >> 4, s = j & 15;
    const int n16 = lane & 15, g = lane >> 4;
    const bool bit3 = (lane & 8) != 0, bit4 = (lane & 16) != 0;
    unsigned* wt = lds + L_WT + wave * (2 * TILE_DW);
    unsigned epoch = 0;
    v4u K[8], V[8];
    v4u W0 = {0, 0, 0, 0}, W1 = W0, W2 = W0, W3 = W0; unsigned xres = 0;
    unsigned stmp;
    {
        const size_t kvrow = (size_t)(b * 8 + kvh) * SK + s * 256;
        const unsigned ko = ((n16 & 7) * DH + ((n16 >> 3) * 4 + g) * 8) * 2, ko2 = ko + 4096;
        const unsigned vo = lane * 16, vo2 = vo + 4096;
        const u16* kb = kc + (kvrow + wave * 16) * DH;
        const u16* vb = vc + (kvrow + wave * 16) * DH;
#define KLD0 "global_load_dwordx4 %0, %[ko], %[kb] nt\n\t"
#define KLD1 "global_load_dwordx4 %1, %[ko], %[kb] offset:128 nt\n\t"
#define KLD2 "global_load_dwordx4 %2, %[ko], %[kb] offset:256 nt\n\t"
#define KLD3 "global_load_dwordx4 %3, %[ko], %[kb] offset:384 nt\n\t"
#define KV_LOADS_A KLD0 KLD1
#define KV_LOADS_A2 KLD2 KLD3
#define KV_LOADS_B \
            "global_load_dwordx4 %4, %[ko2], %[kb] nt\n\tglobal_load_dwordx4 %5, %[ko2], %[kb] offset:128 nt\n\t" \
            "global_load_dwordx4 %6, %[ko2], %[kb] offset:256 nt\n\tglobal_load_dwordx4 %7, %[ko2], %[kb] offset:384 nt\n\t" \
            "global_load_dwordx4 %8, %[vo], %[vb] nt\n\tglobal_load_dwordx4 %9, %[vo], %[vb] offset:1024 nt\n\t" \
            "global_load_dwordx4 %10, %[vo], %[vb] offset:2048 nt\n\tglobal_load_dwordx4 %11, %[vo], %[vb] offset:3072 nt\n\t" \
            "global_load_dwordx4 %12, %[vo2], %[vb] nt\n\tglobal_load_dwordx4 %13, %[vo2], %[vb] offset:1024 nt\n\t" \
            "global_load_dwordx4 %14, %[vo2], %[vb] offset:2048 nt\n\tglobal_load_dwordx4 %15, %[vo2], %[vb] offset:3072 nt\n\t"
#define KV_LOADS KV_LOADS_A KV_LOADS_A2 KV_LOADS_B
#define KV_OUT(c) c(K[0]), c(K[1]), c(K[2]), c(K[3]), c(K[4]), c(K[5]), c(K[6]), c(K[7]), c(V[0]), c(V[1]), c(V[2]), c(V[3]), c(V[4]), c(V[5]), c(V[6]), c(V[7])
        {
            const int i = (wave - 8) & 7;
            const u16* hk = kc + kvrow * DH + (i >> 2) * 8 * DH + (i & 3) * 64;
            const u16* hv = vc + kvrow * DH + i * 512;
            const unsigned lk = (unsigned)(size_t)(__attribute__((address_space(3))) unsigned*)(lds + L_HK + i * 256);
            unsigned keep;
            asm volatile("s_cmp_lt_u32 %[wv], 8\n\ts_cbranch_scc1 1f\n\t"
                         "s_setprio 1\n\t"
                         "s_mov_b32 %[keep], m0\n\ts_mov_b32 m0, %[lk]\n\ts_nop 0\n\tglobal_load_lds_dwordx4 %[ko], %[hk] nt\n\t"
                         "s_add_u32 m0, m0, 0x2000\n\ts_nop 0\n\tglobal_load_lds_dwordx4 %[vo], %[hv] nt\n\ts_mov_b32 m0, %[keep]\n\t"
                         KV_LOADS_A
                         "1:"
                         : KV_OUT("=&v"), [keep] "=&s"(keep)
                         : [wv] "s"(wave), [ko] "v"(ko), [ko2] "v"(ko2), [vo] "v"(vo), [vo2] "v"(vo2), [kb] "s"(kb), [vb] "s"(vb),
                           [hk] "s"(hk), [hv] "s"(hv), [lk] "s"(lk)
                         : "memory", "scc");
        }
        v4u xf = {0, 0, 0, 0}, xa = {0, 0, 0, 0};
        if (wave < 8) {
            const int lc = lane < 56 ? lane : 55;
            const u16* p0 = wave ? wq + (size_t)((j >> 4) * 224 + (wave - 1) * 32 + (lane >> 2)) * NQ + ((kvh + QX) & 7) * 512 + (j & 15) * 32 + (lane & 3) * 8 : x + lc * 8;
            const u16* p1 = wave ? p0 + (size_t)16 * NQ : p0 + DM;
            asm volatile("s_cmp_eq_u32 %[wv], 0\n\ts_cbranch_scc1 1f\n\t"
                         "global_load_dwordx4 %0, %3, off nt\n\tglobal_load_dwordx4 %1, %4, off nt\n\ts_branch 2f\n"
                         "1:\n\tglobal_load_dwordx4 %0, %3, off\n\tglobal_load_dwordx4 %1, %4, off\n"
                         "2:\n\tglobal_load_dword %2, %[zero], %[wsb] sc1"
                         : "=&v"(xf), "=&v"(xa), "+v"(epoch) : "v"(p0), "v"(p1), [wv] "s"(wave), [zero] "v"(0u), [wsb] "s"(ws) : "memory", "scc");
            if (wave == 0) {
                if (lane < 12) lds[L_ZERO + lane] = (lane >= 4 && lane < 8) ? 0x3f803f80u : ((lane == 8 || lane == 9) ? 0xff800000u : 0u);
                if (lane < 16) lds[L_FL + lane] = 0u;
            }
        }
        SCHED();
        asm volatile("s_waitcnt lgkmcnt(0)\n\ts_barrier" ::: "memory");
        asm volatile("s_cmp_lt_u32 %[wv], 8\n\ts_cbranch_scc1 1f\n\t"
                     KV_LOADS_A2
                     "global_load_dword %[ep], %[zero], %[wsb] sc1\n\t"
                     KV_LOADS_B
                     "1:"
                     : KV_OUT("+v"), [ep] "+v"(epoch)
                     : [wv] "s"(wave), [ko] "v"(ko), [ko2] "v"(ko2), [vo] "v"(vo), [vo2] "v"(vo2), [kb] "s"(kb), [vb] "s"(vb), [zero] "v"(0u), [wsb] "s"(ws)
                     : "memory", "scc");
        if (wave < 8) {
            asm volatile("s_waitcnt vmcnt(0)" : "+v"(xf), "+v"(xa), "+v"(epoch));
            const unsigned tag = epoch + 1u;
            if (wave) {
                v4u b0, b1;
                tile_frags(wt, lane, xf, xa, b0, b1);
                asm volatile("" : "+v"(b0), "+v"(b1));
                for (unsigned spins = 0; lds_ld_vol(lds + L_FL) == 0u && spins < (SPIN_CAP << 6); ++spins) __builtin_amdgcn_s_sleep(1);
                CBAR();
                const v4u a0 = *(const v4u*)(lds + L_XN + (n16 & 1) * (DM / 2) + (j >> 4) * 112 + (wave - 1) * 16 + g * 4);
                const v4f z = {0.f, 0.f, 0.f, 0.f};
                const v4f d0 = mfma(a0, b0, z), d1 = mfma(a0, b1, z);
                if (lane < 16) *(v4f*)(ldf + L_QP + ((wave - 1) * 16 + n16) * 4) = (v4f){d0[0], d1[0], d0[1], d1[1]};
                asm volatile("s_waitcnt lgkmcnt(0)" ::: "memory");
                if (lane == 0) __hip_atomic_fetch_add(lds + L_FL + 1, 1u, __ATOMIC_RELAXED, __HIP_MEMORY_SCOPE_WORKGROUP);
            } else {
                const float msk = lane < 56 ? 1.f : 0.f;
                float s0 = 0.f, s1 = 0.f;
#pragma unroll
                for (int k = 0; k < 4; ++k) { s0 += bflo(xf[k]) + bfhi(xf[k]); s1 += bflo(xa[k]) + bfhi(xa[k]); }
                s0 = sum_wave(s0 * msk) * (1.f / DM); s1 = sum_wave(s1 * msk) * (1.f / DM);
                float v0 = 0.f, v1 = 0.f;
#pragma unroll
                for (int k = 0; k < 4; ++k) {
                    float d;
                    d = bflo(xf[k]) - s0; v0 = fmaf(d, d, v0); d = bfhi(xf[k]) - s0; v0 = fmaf(d, d, v0);
                    d = bflo(xa[k]) - s1; v1 = fmaf(d, d, v1); d = bfhi(xa[k]) - s1; v1 = fmaf(d, d, v1);
                }
                v0 = sum_wave(v0 * msk) * (1.f / DM); v1 = sum_wave(v1 * msk) * (1.f / DM);
                float mb0, id0, mb1, id1;
                ln_params(s0, v0, mb0, id0); ln_params(s1, v1, mb1, id1);
                v4u n0, n1;
#pragma unroll
                for (int k = 0; k < 4; ++k) {
                    const unsigned c0 = pkbf(bflo(xf[k]) - mb0, bfhi(xf[k]) - mb0), c1 = pkbf(bflo(xa[k]) - mb1, bfhi(xa[k]) - mb1);
                    n0[k] = pkbf(bflo(c0) * id0, bfhi(c0) * id0);
                    n1[k] = pkbf(bflo(c1) * id1, bfhi(c1) * id1);
                }
                if (lane < 56) { *(v4u*)(lds + L_XN + lane * 4) = n0; *(v4u*)(lds + L_XN + DM / 2 + lane * 4) = n1; }
                asm volatile("s_waitcnt lgkmcnt(0)" ::: "memory");
                if (lane == 0) __hip_atomic_store(lds + L_FL, 1u, __ATOMIC_RELAXED, __HIP_MEMORY_SCOPE_WORKGROUP);
                for (unsigned spins = 0; lds_ld_vol(lds + L_FL + 1) < 7u && spins < (SPIN_CAP << 6); ++spins) {}
                CBAR();
                {
                    v2f t0 = {0.f, 0.f};
#pragma unroll
                    for (int w = 0; w < 7; ++w) t0 += *(const v2f*)(ldf + L_QP + (w * 16 + n16) * 4 + (g & 1) * 2);
                    const v4u gq = {__float_as_uint(t0[0]), tag, __float_as_uint(t0[1]), tag};
                    if (lane < 32) st_g(rs, QG_OFF + ((((kvh + QX) & 7) * 32 + j) * 32 + lane) * 16, gq);
                }
                const unsigned base = QG_OFF + ((kvh * 32 + g) * 32 + b * 16 + n16) * 16;
                v4u gg[8];
                __builtin_amdgcn_s_sleep(8);
                for (unsigned spins = 0;; ++spins) {
                    CBAR();
                    bool ok = true;
#pragma unroll
                    for (int i = 0; i < 8; ++i) gg[i] = ld_g(rs, base + i * 2048);
#pragma unroll
                    for (int i = 0; i < 8; ++i) ok &= tagok(gg[i], tag);
                    if (__all(ok) || spins > SPIN_CAP) break;
                }
#pragma unroll
                for (int m = 0; m < 4; ++m)
                    lds[L_QS + m * 64 + lane] = pkbf(__uint_as_float(gg[m][0]) + __uint_as_float(gg[4 + m][0]), __uint_as_float(gg[m][2]) + __uint_as_float(gg[4 + m][2]));
                asm volatile("s_waitcnt lgkmcnt(0)" ::: "memory");
                if (lane == 0) __hip_atomic_store(lds + L_FL + 2, 1u, __ATOMIC_RELAXED, __HIP_MEMORY_SCOPE_WORKGROUP);
                for (unsigned spins = 0; lds_ld_vol(lds + L_FL + 3) < 8u && spins < (SPIN_CAP << 6); ++spins) __builtin_amdgcn_s_sleep(2);
            }
            __builtin_amdgcn_s_waitcnt(0x0F70);
        }
        asm volatile("s_sub_u32 %[t], %[wv], 1\n\ts_cmp_gt_u32 %[t], 6\n\ts_cbranch_scc1 1f\n\t"
                     "s_setprio 1\n\t"
                     KV_LOADS
                     "1:\n\ts_setprio 0\n\t"
                     "s_cmp_lg_u32 %[wv], 0\n\ts_cbranch_scc1 2f\n\t"
                     "ds_read_b128 %0, %[hl]\n\tds_read_b128 %1, %[hl] offset:1024\n\tds_read_b128 %2, %[hl] offset:2048\n\tds_read_b128 %3, %[hl] offset:3072\n\t"
                     "ds_read_b128 %4, %[hl] offset:4096\n\tds_read_b128 %5, %[hl] offset:5120\n\tds_read_b128 %6, %[hl] offset:6144\n\tds_read_b128 %7, %[hl] offset:7168\n\t"
                     "ds_read_b128 %8, %[hl] offset:8192\n\tds_read_b128 %9, %[hl] offset:9216\n\tds_read_b128 %10, %[hl] offset:10240\n\tds_read_b128 %11, %[hl] offset:11264\n\t"
                     "ds_read_b128 %12, %[hl] offset:12288\n\tds_read_b128 %13, %[hl] offset:13312\n\tds_read_b128 %14, %[hl] offset:14336\n\tds_read_b128 %15, %[hl] offset:15360\n\t"
                     "s_waitcnt lgkmcnt(0)\n\t"
                     "2:"
                     : KV_OUT("+v"), [t] "=&s"(stmp)
                     : [wv] "s"(wave), [ko] "v"(ko), [ko2] "v"(ko2), [vo] "v"(vo), [vo2] "v"(vo2), [kb] "s"(kb), [vb] "s"(vb),
                       [hl] "v"((unsigned)(size_t)(__attribute__((address_space(3))) unsigned*)(lds + L_HK) + lane * 16)
                     : "memory", "scc");
        if (wave >= 8) {
            asm volatile("s_waitcnt vmcnt(17)" ::: "memory");
            if (lane == 0) __hip_atomic_fetch_add(lds + L_FL + 3, 1u, __ATOMIC_RELAXED, __HIP_MEMORY_SCOPE_WORKGROUP);
        }
        for (unsigned spins = 0; lds_ld_vol(lds + L_FL + 2) == 0u && spins < (SPIN_CAP << 6); ++spins) __builtin_amdgcn_s_sleep(2);
        CBAR();
    }
    float t;
    {
        const unsigned* qa = lds + L_QS + (n16 & 1) * 128 + (((n16 >> 1) & 1) * 4 + g) * 4;
        v4f d0 = {0.f, 0.f, 0.f, 0.f}, d1 = {0.f, 0.f, 0.f, 0.f};
        v4u qf[4];
#pragma unroll
        for (int i = 0; i < 4; ++i) qf[i] = *(const v4u*)(qa + i * 32);
        asm volatile("s_waitcnt vmcnt(8)" : "+v"(K[0]), "+v"(K[1]), "+v"(K[2]), "+v"(K[3]), "+v"(K[4]), "+v"(K[5]), "+v"(K[6]), "+v"(K[7]));
#pragma unroll
        for (int i = 0; i < 4; ++i) { d0 = mfma(qf[i], K[i], d0); d1 = mfma(qf[i], K[4 + i], d1); }
        const float s0 = trdpp<ROR8>(bit3 ? d0[2] : d0[0], bit3 ? d1[2] : d1[0], bit3);
        const float s1 = trdpp<ROR8>(bit3 ? d0[3] : d0[1], bit3 ? d1[3] : d1[1], bit3);
        t = rbf(bit4 ? s1 : s0) * (0.0625f * 1.4426950408889634f);
    }
    {
        const float mw = max_row16(t);
        const float p = __builtin_amdgcn_exp2f(t - mw);
        if (lane < 32) ((u16*)(lds + L_PA + wave * 16))[(g * 2 + (n16 & 1)) * 8 + (n16 >> 1)] = (u16)pkbf(p, p);
        CBAR();
        const float lw = sum_row16(p);
        if ((lane & 47) == 0) {
            ldf[L_ML + (lane >> 4) * 16 + wave] = mw; ldf[L_ML + 32 + (lane >> 4) * 16 + wave] = lw;
            __hip_atomic_fetch_max(ldf + L_MF + (lane >> 4), mw, __ATOMIC_RELAXED, __HIP_MEMORY_SCOPE_WORKGROUP);
        }
    }
    v4u PA[4];
#pragma unroll
    for (int tb = 0; tb < 4; ++tb) {
        const bool on = (n16 >> 2) == tb && ((n16 >> 1) & 1) == (g & 1);
        PA[tb] = *(const v4u*)(lds + (on ? L_PA + wave * 16 + ((n16 & 1) * 2 + (g >> 1)) * 4 : L_ZERO));
    }
    {
        v4f acc0 = {0.f, 0.f, 0.f, 0.f}, acc1 = {0.f, 0.f, 0.f, 0.f};
        asm volatile("s_waitcnt vmcnt(0)" : "+v"(V[0]), "+v"(V[1]), "+v"(V[2]), "+v"(V[3]), "+v"(V[4]), "+v"(V[5]), "+v"(V[6]), "+v"(V[7]), "+v"(epoch));
#pragma unroll
        for (int tt = 0; tt < 8; ++tt) {
            v4u bt;
#pragma unroll
            for (int ii = 0; ii < 4; ++ii)
                bt[ii] = (tt & 1) ? phi(V[2 * ii][tt >> 1], V[2 * ii + 1][tt >> 1]) : plo(V[2 * ii][tt >> 1], V[2 * ii + 1][tt >> 1]);
            if (tt < 4) acc0 = mfma(PA[tt & 3], bt, acc0); else acc1 = mfma(PA[tt & 3], bt, acc1);
        }
        *(v4f*)(ldf + L_MB + ((wave * 2 + 0) * 64 + lane) * 4) = acc0;
        *(v4f*)(ldf + L_MB + ((wave * 2 + 1) * 64 + lane) * 4) = acc1;
        asm volatile("s_waitcnt lgkmcnt(0)" ::: "memory");
        if (lane == 0) __hip_atomic_fetch_add(lds + L_MF + 2, 1u, __ATOMIC_RELAXED, __HIP_MEMORY_SCOPE_WORKGROUP);
    }
    const unsigned tag = epoch + 1u;
    if constexpr (ROLE == 1) {
        if (wave >= 4 && wave < 12) {
            const int h = rp0, cc = rp1;
            const u16* p = wo + (size_t)(h * 256 + (wave - 4) * 32 + (lane >> 3)) * DM + cc * 64 + (lane & 7) * 8;
            W0 = ld_nt(p); W1 = ld_nt(p + 8 * DM); W2 = ld_nt(p + 16 * DM); W3 = ld_nt(p + 24 * DM);
        }
    }
    SCHED();
    if (wave < 4) {
        __builtin_amdgcn_s_setprio(3);
        const int lam = wave * 16 + n16;
        unsigned opub = !bit4 ? AG_OFF + ((((kvh * 2 + (g >> 1)) * 2 + b) * 16 + s) * 64 + lam) * 16 : ST_OFF + (((kvh * 2 + (g >> 1)) * 2 + b) * 16 + s) * 128;
        asm volatile("" : "+v"(opub));
        for (unsigned spins = 0; lds_ld_vol(lds + L_MF + 2) < 16u && spins < (SPIN_CAP << 8); ++spins) {}
        CBAR();
        const v2f MM = *(const v2f*)(ldf + L_MF);
        const float mo = ldf[L_ML + (g >> 1) * 16 + n16];
        const float lw = ldf[L_ML + 32 + (g >> 1) * 16 + n16];
        const v4f m0 = *(const v4f*)(ldf + L_ML + 4 * g), m1 = *(const v4f*)(ldf + L_ML + 16 + 4 * g);
        v4f X0[4], X1[4];
#pragma unroll
        for (int k = 0; k < 4; ++k) {
            X0[k] = *(const v4f*)(ldf + L_MB + (((g * 4 + k) * 2 + 0) * 64 + lam) * 4);
            X1[k] = *(const v4f*)(ldf + L_MB + (((g * 4 + k) * 2 + 1) * 64 + lam) * 4);
        }
        const float M0 = MM[0], M1 = MM[1];
        const float M = (g >> 1) ? M1 : M0;
        v4f S0 = {0.f, 0.f, 0.f, 0.f}, S1 = {0.f, 0.f, 0.f, 0.f};
#pragma unroll
        for (int k = 0; k < 4; ++k) {
            const float f0 = __builtin_amdgcn_exp2f(m0[k] - M0), f1 = __builtin_amdgcn_exp2f(m1[k] - M1);
            S0[0] = fmaf(f0, X0[k][0], S0[0]); S0[1] = fmaf(f1, X0[k][1], S0[1]); S0[2] = fmaf(f0, X0[k][2], S0[2]); S0[3] = fmaf(f1, X0[k][3], S0[3]);
            S1[0] = fmaf(f0, X1[k][0], S1[0]); S1[1] = fmaf(f1, X1[k][1], S1[1]); S1[2] = fmaf(f0, X1[k][2], S1[2]); S1[3] = fmaf(f1, X1[k][3], S1[3]);
        }
        const float L = sum_row16(lw * __builtin_amdgcn_exp2f(mo - M));
        const float inv = __builtin_amdgcn_rcpf(L);
        const float e0 = sum16x(tr32(S0[0], S0[1])) * inv, e1 = sum16x(tr32(S1[0], S1[1])) * inv;
        const float e2 = sum16x(tr32(S0[2], S0[3])) * inv, e3 = sum16x(tr32(S1[2], S1[3])) * inv;
        if (!bit4) {
            const v4u gv = {pkbf(e0, e1), tag, pkbf(e2, e3), tag};
            st_g(rs, opub, gv);
        } else if (wave == 0 && n16 == 0) {
            const v2u gs = {__float_as_uint(M + __builtin_amdgcn_logf(L)), tag};
            st_g2(rs, opub, gs);
        }
        __builtin_amdgcn_s_setprio(0);
    }
    v4u W4 = W0, W5 = W0, W6 = W0, W7 = W0;
    if constexpr (ROLE == 2) {
        const int f = rp0;
        const int wr = wave < 14 ? wave : 13;
        {
            const u16* p = w1 + (size_t)(wr * 32 + (lane >> 3)) * FF + f * 64 + (lane & 7) * 8;
            W0 = ld_nt(p); W1 = ld_nt(p + 8 * FF); W2 = ld_nt(p + 16 * FF); W3 = ld_nt(p + 24 * FF);
            const u16* q = w2 + (size_t)(f * 64) * DM + wr * 2048 + lane * 8;
            W4 = ld_nt(q); W5 = ld_nt(q + 512); W6 = ld_nt(q + 1024); W7 = ld_nt(q + 1536);
        }
        xres = *(const GAS u16*)(x + ((lane >> 3) & 1) * DM + wr * 32 + (lane & 7) * 4 + g);
        SCHED();
    }
    if constexpr (ROLE == 1) {
        const int h = rp0, cc = rp1, kk = wave >> 1, bb = wave & 1;
        const unsigned a0 = AG_OFF + ((((h * 2 + bb) * 16 + n16) * 8 + kk) * 8 + 2 * g) * 16;
        const unsigned af = ST_OFF + (h * 32 + (lane & 31)) * 128;
        BARRIER();
        v4u d0 = ld_g(rs, a0), d1 = ld_g(rs, a0 + 16);
        v2u st0 = ld_g2(rs, wave == 15 ? af : 0xFFFFFF00u);
        SCHED();
        if (wave >= 4 && wave < 12) {
            unsigned* w = lds + L_WT + ((wave - 4) * 32 + (lane >> 3)) * 32 + (lane & 7) * 4;
            *(v4u*)w = W0; *(v4u*)(w + 8 * 32) = W1; *(v4u*)(w + 16 * 32) = W2; *(v4u*)(w + 24 * 32) = W3;
            asm volatile("s_waitcnt lgkmcnt(0)" ::: "memory");
            if (lane == 0) __hip_atomic_fetch_add(lds + L_FL + 5, 1u, __ATOMIC_RELAXED, __HIP_MEMORY_SCOPE_WORKGROUP);
        }
        for (unsigned spins = 0; lds_ld_vol(lds + L_FL + 5) < 8u && spins < (SPIN_CAP << 8); ++spins) __builtin_amdgcn_s_sleep(1);
        CBAR();
        v4u B[4];
#pragma unroll
        for (int i = 0; i < 4; ++i) {
            const int lam = 8 * kk + 2 * g + (i >> 1), e = (2 * i) & 3;
            const int row = 128 * (e >> 1) + 8 * (lam & 15) + (lam >> 4);
            const v2u r0 = *(const v2u*)(lds + L_WT + row * 32 + 2 * n16), r1 = *(const v2u*)(lds + L_WT + (row + 4) * 32 + 2 * n16);
            B[0][i] = plo(r0[0], r1[0]); B[1][i] = phi(r0[0], r1[0]);
            B[2][i] = plo(r0[1], r1[1]); B[3][i] = phi(r0[1], r1[1]);
        }
        asm volatile("" : "+v"(B[0]), "+v"(B[1]), "+v"(B[2]), "+v"(B[3]));
        if (wave == 15) {
            for (unsigned spins = 0;; ++spins) {
                const bool ok = st0[1] == tag;
                if (__all(ok) || spins > SPIN_CAP) break;
                CBAR();
                const v2u t2 = ld_g2(rs, ok ? 0xFFFFFF00u : af);
                st0[0] = ok ? st0[0] : t2[0]; st0[1] = ok ? st0[1] : t2[1];
            }
            const float ls = __uint_as_float(st0[0]);
            const float fe = __builtin_amdgcn_exp2f(ls - max_row16(ls));
            const float fw = fe * __builtin_amdgcn_rcpf(sum_row16(fe));
            if (lane < 32) ldf[L_FS + lane] = fw;
            asm volatile("s_waitcnt lgkmcnt(0)" ::: "memory");
            if (lane == 0) __hip_atomic_store(lds + L_FL + 4, 1u, __ATOMIC_RELAXED, __HIP_MEMORY_SCOPE_WORKGROUP);
        }
        for (unsigned spins = 0;; ++spins) {
            const bool ok = tagok(d0, tag) && tagok(d1, tag);
            if (__all(ok) || spins > SPIN_CAP) break;
            __builtin_amdgcn_s_sleep(1);
            CBAR();
            repoll(rs, a0, d0, ok); repoll(rs, a0 + 16, d1, ok);
        }
        const v4u a = {d0[0], d0[2], d1[0], d1[2]};
        const v4f z = {0.f, 0.f, 0.f, 0.f};
        const v4f D0 = mfma(a, B[0], z), D1 = mfma(a, B[1], z), D2 = mfma(a, B[2], z), D3 = mfma(a, B[3], z);
        SCHED();
        for (unsigned spins = 0; lds_ld_vol(lds + L_FL + 4) == 0u && spins < (SPIN_CAP << 8); ++spins) {}
        CBAR();
        const v4f fv = *(const v4f*)(ldf + L_FS + bb * 16 + 4 * g);
        const float f0 = fv[0], f1 = fv[1], f2 = fv[2], f3 = fv[3];
        const float y0 = fmaf(f3, D0[3], fmaf(f2, D0[2], fmaf(f1, D0[1], f0 * D0[0])));
        const float y1 = fmaf(f3, D1[3], fmaf(f2, D1[2], fmaf(f1, D1[1], f0 * D1[0])));
        const float y2 = fmaf(f3, D2[3], fmaf(f2, D2[2], fmaf(f1, D2[1], f0 * D2[0])));
        const float y3 = fmaf(f3, D3[3], fmaf(f2, D3[2], fmaf(f1, D3[1], f0 * D3[0])));
        const float y = tr16(tr32(y0, y2), tr32(y1, y3));
        ldf[L_RP + wave * 64 + 4 * n16 + g] = y;
        BARRIER();
        if (wave == 0) {
            const int b2 = g & 1;
            v4f acc = {0.f, 0.f, 0.f, 0.f};
#pragma unroll
            for (int k = 0; k < 8; ++k) acc += *(const v4f*)(ldf + L_RP + (2 * k + b2) * 64 + 4 * n16);
            const v4u gv = {pkbf(acc[0], acc[1]), tag, pkbf(acc[2], acc[3]), tag};
            if (lane < 32) st_g(rs, YG_OFF + ((((h * 7 + cc) * 2 + (n16 >> 3)) * 2 + b2) * 8 + (n16 & 7)) * 16, gv);
        }
    } else if constexpr (ROLE == 2) {
        const int f = rp0;
        v4u B1[4], B2[4];
        {
            unsigned* w = wt + (lane >> 3) * 32 + (lane & 7) * 4;
            *(v4u*)w = W0; *(v4u*)(w + 272) = W1; *(v4u*)(w + 544) = W2; *(v4u*)(w + 816) = W3;
            CBAR();
            const unsigned* r = wt + g * 272 + 2 * n16;
#pragma unroll
            for (int i = 0; i < 4; ++i) {
                const v2u r0 = *(const v2u*)(r + 64 * i), r1 = *(const v2u*)(r + 64 * i + 32);
                B1[0][i] = plo(r0[0], r1[0]); B1[1][i] = phi(r0[0], r1[0]); B1[2][i] = plo(r0[1], r1[1]); B1[3][i] = phi(r0[1], r1[1]);
            }
            asm volatile("" : "+v"(B1[0]), "+v"(B1[1]), "+v"(B1[2]), "+v"(B1[3]));
        }
        BARRIER();
        if (wave < 14) {
            unsigned* w = lds + L_W2 + wave * 1024 + lane * 4;
            *(v4u*)w = W4; *(v4u*)(w + 256) = W5; *(v4u*)(w + 512) = W6; *(v4u*)(w + 768) = W7;
        }
        BARRIER();
        {
            const unsigned* r = lds + L_W2 + g * 8 * 224 + (wave < 14 ? wave : 13) * 16 + n16;
#pragma unroll
            for (int i = 0; i < 4; ++i) {
                const unsigned a0 = r[(2 * i) * 224], a1 = r[(2 * i + 1) * 224], c0 = r[(32 + 2 * i) * 224], c1 = r[(33 + 2 * i) * 224];
                B2[0][i] = plo(a0, a1); B2[1][i] = phi(a0, a1); B2[2][i] = plo(c0, c1); B2[3][i] = phi(c0, c1);
            }
            asm volatile("" : "+v"(B2[0]), "+v"(B2[1]), "+v"(B2[2]), "+v"(B2[3]), "+v"(xres));
        }
        const v4f z4 = {0.f, 0.f, 0.f, 0.f};
        unsigned klo = 0x00003f80u, khi = 0x3f800000u;
        asm volatile("" : "+v"(klo), "+v"(khi));
        if (wave < 14) {
            const unsigned ay = YG_OFF + (((g * 7 + (wave >> 1)) * 2 + (wave & 1)) * 16 + n16) * 16;
            constexpr unsigned HS = 4 * 7 * 512;
            v4u y[4];
#pragma unroll
            for (int i = 0; i < 4; ++i) y[i] = ld_g(rs, ay + i * HS);
            for (unsigned spins = 0;; ++spins) {
                const bool k0 = tagok(y[0], tag), k1 = tagok(y[1], tag), k2 = tagok(y[2], tag), k3 = tagok(y[3], tag);
                if (__all(k0 && k1 && k2 && k3) || spins > SPIN_CAP) break;
                __builtin_amdgcn_s_sleep(1);
                CBAR();
                repoll(rs, ay, y[0], k0); repoll(rs, ay + HS, y[1], k1); repoll(rs, ay + 2 * HS, y[2], k2); repoll(rs, ay + 3 * HS, y[3], k3);
            }
            float c0 = 0.f, c1 = 0.f, c2 = 0.f, c3 = 0.f;
#pragma unroll
            for (int i = 0; i < 4; ++i) {
                c0 = dot2(y[i][0], klo, c0); c1 = dot2(y[i][0], khi, c1);
                c2 = dot2(y[i][2], klo, c2); c3 = dot2(y[i][2], khi, c3);
            }
            const float ys = tr16(tr32(c0, c2), tr32(c1, c3));
            const unsigned hb16 = pkbf(bflo(xres) + rbf(ys), 0.f);
            ((u16*)(lds + L_HB))[(bit3 ? DM : 0) + wave * 32 + (lane & 7) * 4 + g] = (u16)hb16;
            CBAR();
            const v4u av = *(const v4u*)(lds + (n16 == 2 ? L_ONES : L_HB + (n16 & 1) * (DM / 2) + wave * 16 + g * 4));
            const v4f D0 = mfma(av, B1[0], z4), D1 = mfma(av, B1[1], z4), D2 = mfma(av, B1[2], z4), D3 = mfma(av, B1[3], z4);
            const v4f DS = mfma(av, av, z4);
            if (lane < 2) *(v2f*)(ldf + L_ST + (lane * 16 + wave) * 2) = (v2f){DS[2], lane ? DS[1] : DS[0]};
            if (lane < 16) {
                float* zp = ldf + L_RP + (wave * 64 + n16) * 4;
                *(v4f*)zp = D0; *(v4f*)(zp + 64) = D1; *(v4f*)(zp + 128) = D2; *(v4f*)(zp + 192) = D3;
            }
        }
        BARRIER();
        if (wave == 15) {
            const v4u gh = {lds[L_HB + f * 8 + (lane & 7)], tag, lds[L_HB + DM / 2 + f * 8 + (lane & 7)], tag};
            if (lane < 8) st_g(rs, OG_OFF + (NF * 224 + f * 8 + lane) * 16, gh);
        }
        if (wave < 4) {
            const float* zp = ldf + L_RP + (wave * 16 + n16) * 4;
            const v2f sv = *(const v2f*)(n16 < 14 ? ldf + L_ST + ((g >> 1) * 16 + n16) * 2 : ldf + L_ZERO);
            v4f zs = *(const v4f*)(zp + g * 256) + *(const v4f*)(zp + (g + 4) * 256) + *(const v4f*)(zp + (g + 8) * 256);
            const v4f zl = *(const v4f*)(g < 2 ? zp + (g + 12) * 256 : ldf + L_ZERO);
            zs += zl;
            const float mean = sum_row16(sv[0]) * (1.f / DM);
            const float var = fmaf(-mean, mean, sum_row16(sv[1]) * (1.f / DM));
            float mb, idn;
            ln_params(mean, fmaxf(var, 0.f), mb, idn);
            const float zb = sum16x(tr32(zs[0], zs[1])), cs = sum16x(sum32x(zs[2]));
            const float zz = rbf((zb - mb * cs) * idn);
            const float act = zz * __builtin_amdgcn_rcpf(1.f + __builtin_amdgcn_exp2f(zz * -1.4426950408889634f));
            if (!bit4) ((u16*)(lds + L_AA))[(g >> 1) * 64 + 4 * n16 + wave] = (u16)pkbf(act, act);
        }
        BARRIER();
        if (wave < 14) {
            __builtin_amdgcn_s_setprio(3);
            const v4u a0 = *(const v4u*)(lds + L_AA + (n16 & 1) * 32 + g * 4), a1 = *(const v4u*)(lds + L_AA + (n16 & 1) * 32 + 16 + g * 4);
            const v4f D0 = mfma(a1, B2[2], mfma(a0, B2[0], z4)), D1 = mfma(a1, B2[3], mfma(a0, B2[1], z4));
            const v4u g0 = {pkbf(D0[0], D1[0]), tag, pkbf(D0[1], D1[1]), tag};
            if (lane < 16) st_g(rs, OG_OFF + (f * 224 + wave * 16 + n16) * 16, g0);
            __builtin_amdgcn_s_setprio(0);
        }
    } else if constexpr (ROLE == 3) {
        if (wave == 15) {
            const int f = rp0;
            unsigned klo = 0x00003f80u, khi = 0x3f800000u;
            asm volatile("" : "+v"(klo), "+v"(khi));
            const unsigned ah = OG_OFF + (NF * 224 + f * 8 + (lane & 7)) * 16;
            v4u gh;
            for (unsigned spins = 0;; ++spins) {
                CBAR();
                gh = ld_g(rs, ah);
                if (__all(tagok(gh, tag)) || spins > SPIN_CAP) break;
                __builtin_amdgcn_s_sleep(6);
            }
            const unsigned hh2 = lane < 32 ? gh[0] : gh[2];
            const unsigned a = OG_OFF + ((lane >> 3) * 224 + f * 8 + (lane & 7)) * 16;
            constexpr unsigned FS = 8 * 224 * 16;
            const int col = (lane >> 5) * DM + f * 16 + 2 * (lane & 7);
            v4u gv[4];
#pragma unroll
            for (int i = 0; i < 3; ++i) gv[i] = ld_g(rs, a + i * FS);
            const unsigned a3 = lane < 32 ? a + 3 * FS : a;
            gv[3] = ld_g(rs, a3);
            for (unsigned spins = 0;; ++spins) {
                const bool k0 = tagok(gv[0], tag), k1 = tagok(gv[1], tag), k2 = tagok(gv[2], tag), k3 = tagok(gv[3], tag);
                if (__all(k0 && k1 && k2 && k3) || spins > SPIN_CAP) break;
                CBAR();
                __builtin_amdgcn_s_sleep(1);
                repoll(rs, a, gv[0], k0); repoll(rs, a + FS, gv[1], k1); repoll(rs, a + 2 * FS, gv[2], k2); repoll(rs, a3, gv[3], k3);
            }
            if (lane >= 32) { gv[3][0] = 0u; gv[3][2] = 0u; }
            float c0 = 0.f, c1 = 0.f, c2 = 0.f, c3 = 0.f;
#pragma unroll
            for (int i = 0; i < 4; ++i) {
                c0 = dot2(gv[i][0], klo, c0); c1 = dot2(gv[i][0], khi, c1);
                c2 = dot2(gv[i][2], klo, c2); c3 = dot2(gv[i][2], khi, c3);
            }
            float s0 = sum16x(tr32(c0, c2)), s1 = sum16x(tr32(c1, c3));
            s0 += dpp<ROR8>(s0); s1 += dpp<ROR8>(s1);
            if ((lane & 24) == 0) {
                *(GAS unsigned*)(out + col) = pkbf(bflo(hh2) + rbf(s0), bfhi(hh2) + rbf(s1));
                if (f == 0 && lane == 0 ) __hip_atomic_store((GAS unsigned*)(ws + E_OFF), tag, __ATOMIC_RELAXED, __HIP_MEMORY_SCOPE_AGENT);
            }
        }
    }
}

__global__ void __launch_bounds__(NTHR, 4)
fused_decode(const u16* __restrict__ x, const u16* __restrict__ kc, const u16* __restrict__ vc, const u16* __restrict__ wq,
             const u16* __restrict__ wo, const u16* __restrict__ w1, const u16* __restrict__ w2, u16* __restrict__ out, char* ws) {
    __shared__ __attribute__((aligned(16))) unsigned lds[L_END];
    const int wave = __builtin_amdgcn_readfirstlane(threadIdx.x >> 6), lane = threadIdx.x & 63;
    const int kvh = blockIdx.x & 7, j = blockIdx.x >> 3;
    int role = 0, rp0 = 0, rp1 = 0;
    role_of(kvh, j, role, rp0, rp1);
    if (role == 1) body<1>(x, kc, vc, wq, wo, w1, w2, out, ws, lds, kvh, j, rp0, rp1, wave, lane);
    else if (role == 2) body<2>(x, kc, vc, wq, wo, w1, w2, out, ws, lds, kvh, j, rp0, rp1, wave, lane);
    else if (role == 3) body<3>(x, kc, vc, wq, wo, w1, w2, out, ws, lds, kvh, j, rp0, rp1, wave, lane);
    else body<0>(x, kc, vc, wq, wo, w1, w2, out, ws, lds, kvh, j, rp0, rp1, wave, lane);
}

extern "C" void kernel_launch(void* const* d_in, const int* in_sizes, int n_in, void* d_out, int out_size, void* d_ws,
                              size_t ws_size, hipStream_t stream) {
    fused_decode<<<256, NTHR, 0, stream>>>((const u16*)d_in[0], (const u16*)d_in[1], (const u16*)d_in[2], (const u16*)d_in[3],
                                           (const u16*)d_in[4], (const u16*)d_in[6], (const u16*)d_in[7], (u16*)d_out, (char*)d_ws);
}
```
